```python
import torch

def _softmax(x, axis=-1):
    return torch.softmax(x, dim=axis)
def _gelu(x):
    return torch.nn.functional.gelu(x, approximate="tanh")
def _silu(x):
    return torch.nn.functional.silu(x)
def _sigmoid(x):
    return torch.sigmoid(x)
def _layernorm(x, axis=-1, eps=1e-5):
    m = x.mean(dim=axis, keepdim=True)
    v = x.var(dim=axis, keepdim=True, unbiased=False)
    return (x - m) / torch.sqrt(v + eps)
def _scatter_add(tgt, idx, src, axis=0):
    return tgt.clone().index_add_(axis, idx, src)
def _segment_sum(src, seg, n):
    out = torch.zeros((n,) + src.shape[1:], dtype=src.dtype)
    return out.index_add_(0, seg, src)
def _pad(x, pw):
    flat = tuple(v for pair in reversed(pw) for v in pair)
    return torch.nn.functional.pad(x, flat)

kernel_name = 'interleave_layers__gqa_block_cached__glu_gated_glu__decode'

def setup_inputs():

    _w = (lambda a: (lambda t: t.to(torch.bfloat16) if t.is_floating_point() else t)(torch.as_tensor(a)))
    def _A_setup():
        B, Sq, D = 8, 1, 512
        Hq, Hkv, Sk, Dh = 32, 32, 4096, 128
        assert Hq % Hkv == 0, (Hq, Hkv)
        return {
            "x": _w((lambda *s: torch.randn(*s, dtype=torch.bfloat16) * 0.05)(B, Sq, D)),
            "k_cache": _w((lambda *s: torch.randn(*s, dtype=torch.bfloat16) * 1.6)(B, Hkv, Sk, Dh)),
            "v_cache": _w((lambda *s: torch.randn(*s, dtype=torch.bfloat16) * 1.6)(B, Hkv, Sk, Dh)),
            "wq": _w((lambda *s: torch.randn(*s, dtype=torch.bfloat16) * 1.6)(D, Hq * Dh) * (D ** -0.5)),
            "wo": _w((lambda *s: torch.randn(*s, dtype=torch.bfloat16) * 1.6)(Hq * Dh, D) * ((Hq * Dh) ** -0.5)),
        }
    _A_in = _A_setup()

    def _B_setup():
        B, S, D, F = 8, 1, 512, 1376
        return {
            "x": _w((lambda *s: torch.randn(*s, dtype=torch.bfloat16) * 0.05)(B, S, D)),
            "w_gate": _w((lambda *s: torch.randn(*s, dtype=torch.bfloat16) * 0.05)(D, F)),
            "w_up": _w((lambda *s: torch.randn(*s, dtype=torch.bfloat16) * 0.05)(D, F)),
            "w_down": _w((lambda *s: torch.randn(*s, dtype=torch.bfloat16) * 0.05)(F, D)),
        }
    _B_in = _B_setup()

    return {
        "A_x": _A_in["x"],
        "A_k_cache": _A_in["k_cache"],
        "A_v_cache": _A_in["v_cache"],
        "A_wq": _A_in["wq"],
        "A_wo": _A_in["wo"],
        "B_x": _B_in["x"],
        "B_w_gate": _B_in["w_gate"],
        "B_w_up": _B_in["w_up"],
        "B_w_down": _B_in["w_down"],
    }

def reference(A_x, A_k_cache, A_v_cache, A_wq, A_wo, B_x, B_w_gate, B_w_up, B_w_down):
    def _A_ref(x, k_cache, v_cache, wq, wo):
        B, Sq, D = x.shape
        Hq, Dh = 32, 128
        Hkv = k_cache.shape[1]
        rep = Hq // Hkv
        q = (lambda x, *d: x.permute(*d))((x @ wq).reshape(B, Sq, Hq, Dh), 0, 2, 1, 3)
        k = (lambda x, n, axis: torch.repeat_interleave(x, n, dim=axis))(k_cache, rep, axis=1)
        v = (lambda x, n, axis: torch.repeat_interleave(x, n, dim=axis))(v_cache, rep, axis=1)
        scores = torch.einsum("bhqd,bhkd->bhqk", q, k) * (Dh ** -0.5)
        p = _softmax(scores, axis=-1)
        o = torch.einsum("bhqk,bhkd->bhqd", p, v)
        return (lambda x, *d: x.permute(*d))(o, 0, 2, 1, 3).reshape(B, Sq, Hq * Dh) @ wo

    def _B_ref(x, w_gate, w_up, w_down):
        g = torch.einsum("bsd,df->bsf", x, w_gate)
        u = torch.einsum("bsd,df->bsf", x, w_up)
        h = _sigmoid(g) * u
        return torch.einsum("bsf,fd->bsd", h, w_down)

    x = A_x
    x = x + _A_ref(_layernorm(x, axis=-1), A_k_cache, A_v_cache, A_wq, A_wo)
    x = x + _B_ref(_layernorm(x, axis=-1), B_w_gate, B_w_up, B_w_down)
    x = x + _A_ref(_layernorm(x, axis=-1), A_k_cache, A_v_cache, A_wq, A_wo)
    x = x + _B_ref(_layernorm(x, axis=-1), B_w_gate, B_w_up, B_w_down)
    return x
```

```cpp
#include <hip/hip_runtime.h>
#include <hip/hip_bf16.h>
#include <stdint.h>

typedef unsigned int u32;
typedef unsigned short u16;
using u32x4 = __attribute__((ext_vector_type(4))) unsigned;
using bf16x2 = __attribute__((ext_vector_type(2))) __bf16;

constexpr int B = 8, D = 512, H = 32, SK = 4096, DH = 128, F = 1376;
constexpr int NSPLIT = 4;

__device__ __forceinline__ float bf2f(u16 v) { return __uint_as_float(((u32)v) << 16); }
__device__ __forceinline__ float bflo(u32 v) { return __uint_as_float(v << 16); }
__device__ __forceinline__ float bfhi(u32 v) { return __uint_as_float(v & 0xffff0000u); }
__device__ __forceinline__ u16 f2bf(float f) { __hip_bfloat16 h = __float2bfloat16(f); return *reinterpret_cast<u16*>(&h); }
__device__ __forceinline__ float dot2(u32 k, u32 q, float acc) {
  return __builtin_amdgcn_fdot2_f32_bf16(__builtin_bit_cast(bf16x2, k), __builtin_bit_cast(bf16x2, q), acc, false);
}

__device__ void ln_all(const float* x, float* xn  , int tid) {
  int wave = tid >> 6, lane = tid & 63;
  for (int b = wave; b < B; b += 4) {
    float v[8]; float s = 0;
    for (int i = 0; i < 8; ++i) { v[i] = x[b * D + lane + 64 * i]; s += v[i]; }
    for (int o = 32; o; o >>= 1) s += __shfl_xor(s, o);
    float m = s / D; float q = 0;
    for (int i = 0; i < 8; ++i) { float d = v[i] - m; q += d * d; }
    for (int o = 32; o; o >>= 1) q += __shfl_xor(q, o);
    float r = rsqrtf(q / D + 1e-5f);
    for (int i = 0; i < 8; ++i) xn[b * D + lane + 64 * i] = bf2f(f2bf((v[i] - m) * r));
  }
  __syncthreads();
}

__global__ void k_init(const u16* x, float* xf) { int i = blockIdx.x * 256 + threadIdx.x; if (i < B * D) xf[i] = bf2f(x[i]); }

__global__ void __launch_bounds__(256) k_q(const float* x, const u16* wq, u16* q) {
  __shared__ float xn[B * D];
  __shared__ float red[4][B][64];
  int tid = threadIdx.x; ln_all(x, xn, tid);
  int wave = tid >> 6, lane = tid & 63; int n = blockIdx.x * 64 + lane;
  float acc[B] = {};
  for (int k = wave * 128; k < wave * 128 + 128; ++k) { float w = bf2f(wq[(size_t)k * 4096 + n]);
    for (int b = 0; b < B; ++b) acc[b] += xn[b * D + k] * w; }
  for (int b = 0; b < B; ++b) red[wave][b][lane] = acc[b];
  __syncthreads();
  for (int i = tid; i < B * 64; i += 256) { int b = i >> 6, c = i & 63;
    q[b * 4096 + blockIdx.x * 64 + c] = f2bf(red[0][b][c] + red[1][b][c] + red[2][b][c] + red[3][b][c]); }
}

__global__ void __launch_bounds__(256) k_attn(const u16* q, const u16* K, const u16* V, float* pm, float* po) {
  __shared__ float sm[4][4], sl[4][4]; __shared__ float so[4][4][DH];
  int s = blockIdx.x, h = blockIdx.y, b = blockIdx.z;
  int tid = threadIdx.x, lane = tid & 63, wave = tid >> 6, r = lane >> 4, c = lane & 15;
  const float C2 = 0.08838834764831845f * 1.4426950408889634f;
  u32x4 qp = *(const u32x4*)(q + b * 4096 + h * DH + 8 * c);
  const u16* kb = K + ((size_t)(b * H + h) * SK) * DH; const u16* vb = V + ((size_t)(b * H + h) * SK) * DH;
  constexpr int KPS = SK / NSPLIT;
  float m = -INFINITY, l = 0, acc[8] = {};
  for (int k0 = s * KPS + wave * 16; k0 < (s + 1) * KPS; k0 += 64) {
    u32x4 kk[4], vv[4];
    for (int t = 0; t < 4; ++t) { kk[t] = __builtin_nontemporal_load((const u32x4*)(kb + (size_t)(k0 + 4 * t + r) * DH + 8 * c));
                                  vv[t] = __builtin_nontemporal_load((const u32x4*)(vb + (size_t)(k0 + 4 * t + r) * DH + 8 * c)); }
    float sc[4];
    for (int t = 0; t < 4; ++t) { float d = 0; for (int j = 0; j < 4; ++j) d = dot2(kk[t][j], qp[j], d);
      d += __shfl_xor(d, 1); d += __shfl_xor(d, 2); d += __shfl_xor(d, 4); d += __shfl_xor(d, 8); sc[t] = d * C2; }
    float mn = fmaxf(fmaxf(fmaxf(sc[0], sc[1]), fmaxf(sc[2], sc[3])), m);
    float al = exp2f(m - mn); m = mn; l *= al;
    for (int j = 0; j < 8; ++j) acc[j] *= al;
    for (int t = 0; t < 4; ++t) { float p = exp2f(sc[t] - mn); l += p;
      for (int j = 0; j < 4; ++j) { acc[2 * j] += p * bflo(vv[t][j]); acc[2 * j + 1] += p * bfhi(vv[t][j]); } }
  }
  if (c == 0) { sm[wave][r] = m; sl[wave][r] = l; }
  for (int j = 0; j < 8; ++j) so[wave][r][8 * c + j] = acc[j];
  __syncthreads();
  if (tid < DH) {
    float M = -INFINITY; for (int i = 0; i < 16; ++i) M = fmaxf(M, sm[i >> 2][i & 3]);
    float L = 0, o = 0; for (int i = 0; i < 16; ++i) { float w = exp2f(sm[i >> 2][i & 3] - M); L += w * sl[i >> 2][i & 3]; o += w * so[i >> 2][i & 3][tid]; }
    size_t slot = (size_t)(b * H + h) * NSPLIT + s;
    po[slot * DH + tid] = o; if (tid == 0) { pm[slot * 2] = M; pm[slot * 2 + 1] = L; }
  }
}

__global__ void __launch_bounds__(256) k_wo(const float* pm, const float* po, const u16* wo, float* yp) {
  __shared__ float o[B][DH];
  int h = blockIdx.x, tid = threadIdx.x;
  for (int i = tid; i < B * DH; i += 256) { int b = i >> 7, d = i & 127; size_t s0 = (size_t)(b * H + h) * NSPLIT;
    float M = -INFINITY; for (int s = 0; s < NSPLIT; ++s) M = fmaxf(M, pm[(s0 + s) * 2]);
    float L = 0, a = 0; for (int s = 0; s < NSPLIT; ++s) { float w = exp2f(pm[(s0 + s) * 2] - M); L += w * pm[(s0 + s) * 2 + 1]; a += w * po[(s0 + s) * DH + d]; }
    o[b][d] = bf2f(f2bf(a / L)); }
  __syncthreads();
  float a0[B] = {}, a1[B] = {};
  for (int d = 0; d < DH; ++d) { u32 w = *(const u32*)(wo + (size_t)(h * DH + d) * D + 2 * tid);
    for (int b = 0; b < B; ++b) { a0[b] += o[b][d] * bflo(w); a1[b] += o[b][d] * bfhi(w); } }
  for (int b = 0; b < B; ++b) { yp[(h * B + b) * D + 2 * tid] = a0[b]; yp[(h * B + b) * D + 2 * tid + 1] = a1[b]; }
}
__global__ void k_sum(const float* part, int n, float* x, u16* out) { int i = blockIdx.x * 256 + threadIdx.x; if (i >= B * D) return;
  float a = 0; for (int j = 0; j < n; ++j) a += part[j * B * D + i]; a += x[i]; x[i] = a; if (out) out[i] = f2bf(a); }

__global__ void __launch_bounds__(256) k_ffn(const float* x, const u16* wg, const u16* wu, const u16* wd, float* zp) {
  __shared__ float xn[B * D]; __shared__ float red[2][8][B][32]; __shared__ float hh[B][32];
  int tid = threadIdx.x; ln_all(x, xn, tid);
  int ks = tid >> 5, c = tid & 31, f = blockIdx.x * 32 + c;
  float g[B] = {}, u[B] = {};
  for (int k = ks * 64; k < ks * 64 + 64; ++k) { float a = bf2f(wg[(size_t)k * F + f]), bb = bf2f(wu[(size_t)k * F + f]);
    for (int b = 0; b < B; ++b) { g[b] += xn[b * D + k] * a; u[b] += xn[b * D + k] * bb; } }
  for (int b = 0; b < B; ++b) { red[0][ks][b][c] = g[b]; red[1][ks][b][c] = u[b]; }
  __syncthreads();
  { int b = tid >> 5; float gg = 0, uu = 0; for (int j = 0; j < 8; ++j) { gg += red[0][j][b][c]; uu += red[1][j][b][c]; }
    hh[b][c] = bf2f(f2bf(uu / (1.f + __expf(-gg)))); }
  __syncthreads();
  float a0[B] = {}, a1[B] = {};
  for (int j = 0; j < 32; ++j) { u32 w = *(const u32*)(wd + (size_t)(blockIdx.x * 32 + j) * D + 2 * tid);
    for (int b = 0; b < B; ++b) { a0[b] += hh[b][j] * bflo(w); a1[b] += hh[b][j] * bfhi(w); } }
  for (int b = 0; b < B; ++b) { zp[(blockIdx.x * B + b) * D + 2 * tid] = a0[b]; zp[(blockIdx.x * B + b) * D + 2 * tid + 1] = a1[b]; }
}

extern "C" void kernel_launch(void* const* d_in, const int* in_sizes, int n_in, void* d_out, int out_size, void* d_ws, size_t ws_size, hipStream_t stream) {
  const u16* x = (const u16*)d_in[0]; const u16* K = (const u16*)d_in[1]; const u16* V = (const u16*)d_in[2];
  const u16* wq = (const u16*)d_in[3]; const u16* wo = (const u16*)d_in[4];
  const u16* wg = (const u16*)d_in[6]; const u16* wu = (const u16*)d_in[7]; const u16* wd = (const u16*)d_in[8];
  char* ws = (char*)d_ws;
  float* xf = (float*)ws; u16* q = (u16*)(ws + 65536); float* pm = (float*)(ws + 2 * 65536 * 2); float* po = (float*)(ws + (1 << 20)); float* part = (float*)(ws + (4 << 20));
  k_init<<<16, 256, 0, stream>>>(x, xf);
  for (int l = 0; l < 2; ++l) {
    k_q<<<64, 256, 0, stream>>>(xf, wq, q);
    k_attn<<<dim3(NSPLIT, H, B), 256, 0, stream>>>(q, K, V, pm, po);
    k_wo<<<32, 256, 0, stream>>>(pm, po, wo, part);
    k_sum<<<16, 256, 0, stream>>>(part, 32, xf, nullptr);
    k_ffn<<<43, 256, 0, stream>>>(xf, wg, wu, wd, part);
    k_sum<<<16, 256, 0, stream>>>(part, 43, xf, l == 1 ? (u16*)d_out : nullptr);
  }
}
```

```cpp
#include <hip/hip_runtime.h>
#include <hip/hip_bf16.h>
#include <stdint.h>

typedef unsigned int u32;
typedef unsigned short u16;
using u32x4 = __attribute__((ext_vector_type(4))) unsigned;
using bf16x2 = __attribute__((ext_vector_type(2))) __bf16;

constexpr int B = 8, D = 512, H = 32, SK = 4096, DH = 128, F = 1376;
constexpr int NSPLIT = 4;

__device__ __forceinline__ float bf2f(u16 v) { return __uint_as_float(((u32)v) << 16); }
__device__ __forceinline__ float bflo(u32 v) { return __uint_as_float(v << 16); }
__device__ __forceinline__ float bfhi(u32 v) { return __uint_as_float(v & 0xffff0000u); }
__device__ __forceinline__ u16 f2bf(float f) { __hip_bfloat16 h = __float2bfloat16(f); return *reinterpret_cast<u16*>(&h); }
__device__ __forceinline__ float rb(float f) { return bf2f(f2bf(f)); }
__device__ __forceinline__ float dot2(u32 k, u32 q, float acc) {
  return __builtin_amdgcn_fdot2_f32_bf16(__builtin_bit_cast(bf16x2, k), __builtin_bit_cast(bf16x2, q), acc, false);
}

__device__ void ln_all(const float* x, float* xn  , int tid) {
  int wave = tid >> 6, lane = tid & 63;
  for (int b = wave; b < B; b += 4) {
    float v[8]; float s = 0;
    for (int i = 0; i < 8; ++i) { v[i] = x[b * D + lane + 64 * i]; s += v[i]; }
    for (int o = 32; o; o >>= 1) s += __shfl_xor(s, o);
    float m = s / D; float q = 0;
    for (int i = 0; i < 8; ++i) { float d = v[i] - m; q += d * d; }
    for (int o = 32; o; o >>= 1) q += __shfl_xor(q, o);
    float mb = rb(m), sd = rb(sqrtf(rb(rb(q / D) + 1e-5f)));
    for (int i = 0; i < 8; ++i) xn[b * D + lane + 64 * i] = rb(rb(v[i] - mb) / sd);
  }
  __syncthreads();
}

__global__ void k_init(const u16* x, float* xf) { int i = blockIdx.x * 256 + threadIdx.x; if (i < B * D) xf[i] = bf2f(x[i]); }

__global__ void __launch_bounds__(256) k_q(const float* x, const u16* wq, u16* q) {
  __shared__ float xn[B * D];
  __shared__ float red[4][B][64];
  int tid = threadIdx.x; ln_all(x, xn, tid);
  int wave = tid >> 6, lane = tid & 63; int n = blockIdx.x * 64 + lane;
  float acc[B] = {};
  for (int k = wave * 128; k < wave * 128 + 128; ++k) { float w = bf2f(wq[(size_t)k * 4096 + n]);
    for (int b = 0; b < B; ++b) acc[b] += xn[b * D + k] * w; }
  for (int b = 0; b < B; ++b) red[wave][b][lane] = acc[b];
  __syncthreads();
  for (int i = tid; i < B * 64; i += 256) { int b = i >> 6, c = i & 63;
    q[b * 4096 + blockIdx.x * 64 + c] = f2bf(red[0][b][c] + red[1][b][c] + red[2][b][c] + red[3][b][c]); }
}

__global__ void __launch_bounds__(256) k_attn(const u16* q, const u16* K, const u16* V, float* pm, float* po) {
  __shared__ float sm[4][4], sl[4][4]; __shared__ float so[4][4][DH];
  int s = blockIdx.x, h = blockIdx.y, b = blockIdx.z;
  int tid = threadIdx.x, lane = tid & 63, wave = tid >> 6, r = lane >> 4, c = lane & 15;
  const float C2 = 0.08838834764831845f * 1.4426950408889634f;
  u32x4 qp = *(const u32x4*)(q + b * 4096 + h * DH + 8 * c);
  const u16* kb = K + ((size_t)(b * H + h) * SK) * DH; const u16* vb = V + ((size_t)(b * H + h) * SK) * DH;
  constexpr int KPS = SK / NSPLIT;
  float m = -INFINITY, l = 0, acc[8] = {};
  for (int k0 = s * KPS + wave * 16; k0 < (s + 1) * KPS; k0 += 64) {
    u32x4 kk[4], vv[4];
    for (int t = 0; t < 4; ++t) { kk[t] = __builtin_nontemporal_load((const u32x4*)(kb + (size_t)(k0 + 4 * t + r) * DH + 8 * c));
                                  vv[t] = __builtin_nontemporal_load((const u32x4*)(vb + (size_t)(k0 + 4 * t + r) * DH + 8 * c)); }
    float sc[4];
    for (int t = 0; t < 4; ++t) { float d = 0; for (int j = 0; j < 4; ++j) d = dot2(kk[t][j], qp[j], d);
      d += __shfl_xor(d, 1); d += __shfl_xor(d, 2); d += __shfl_xor(d, 4); d += __shfl_xor(d, 8); sc[t] = rb(rb(d) * 0.08838834764831845f) * 1.4426950408889634f; }
    float mn = fmaxf(fmaxf(fmaxf(sc[0], sc[1]), fmaxf(sc[2], sc[3])), m);
    float al = exp2f(m - mn); m = mn; l *= al;
    for (int j = 0; j < 8; ++j) acc[j] *= al;
    for (int t = 0; t < 4; ++t) { float p = exp2f(sc[t] - mn); l += p;
      for (int j = 0; j < 4; ++j) { acc[2 * j] += p * bflo(vv[t][j]); acc[2 * j + 1] += p * bfhi(vv[t][j]); } }
  }
  if (c == 0) { sm[wave][r] = m; sl[wave][r] = l; }
  for (int j = 0; j < 8; ++j) so[wave][r][8 * c + j] = acc[j];
  __syncthreads();
  if (tid < DH) {
    float M = -INFINITY; for (int i = 0; i < 16; ++i) M = fmaxf(M, sm[i >> 2][i & 3]);
    float L = 0, o = 0; for (int i = 0; i < 16; ++i) { float w = exp2f(sm[i >> 2][i & 3] - M); L += w * sl[i >> 2][i & 3]; o += w * so[i >> 2][i & 3][tid]; }
    size_t slot = (size_t)(b * H + h) * NSPLIT + s;
    po[slot * DH + tid] = o; if (tid == 0) { pm[slot * 2] = M; pm[slot * 2 + 1] = L; }
  }
}

__global__ void __launch_bounds__(256) k_wo(const float* pm, const float* po, const u16* wo, float* yp) {
  __shared__ float o[B][DH];
  int h = blockIdx.x, tid = threadIdx.x;
  for (int i = tid; i < B * DH; i += 256) { int b = i >> 7, d = i & 127; size_t s0 = (size_t)(b * H + h) * NSPLIT;
    float M = -INFINITY; for (int s = 0; s < NSPLIT; ++s) M = fmaxf(M, pm[(s0 + s) * 2]);
    float L = 0, a = 0; for (int s = 0; s < NSPLIT; ++s) { float w = exp2f(pm[(s0 + s) * 2] - M); L += w * pm[(s0 + s) * 2 + 1]; a += w * po[(s0 + s) * DH + d]; }
    o[b][d] = bf2f(f2bf(a / L)); }
  __syncthreads();
  float a0[B] = {}, a1[B] = {};
  for (int d = 0; d < DH; ++d) { u32 w = *(const u32*)(wo + (size_t)(h * DH + d) * D + 2 * tid);
    for (int b = 0; b < B; ++b) { a0[b] += o[b][d] * bflo(w); a1[b] += o[b][d] * bfhi(w); } }
  for (int b = 0; b < B; ++b) { yp[(h * B + b) * D + 2 * tid] = a0[b]; yp[(h * B + b) * D + 2 * tid + 1] = a1[b]; }
}
__global__ void k_sum(const float* part, int n, float* x, u16* out) { int i = blockIdx.x * 256 + threadIdx.x; if (i >= B * D) return;
  float a = 0; for (int j = 0; j < n; ++j) a += part[j * B * D + i]; a = rb(rb(a) + x[i]); x[i] = a; if (out) out[i] = f2bf(a); }

__global__ void __launch_bounds__(256) k_ffn(const float* x, const u16* wg, const u16* wu, const u16* wd, float* zp) {
  __shared__ float xn[B * D]; __shared__ float red[2][8][B][32]; __shared__ float hh[B][32];
  int tid = threadIdx.x; ln_all(x, xn, tid);
  int ks = tid >> 5, c = tid & 31, f = blockIdx.x * 32 + c;
  float g[B] = {}, u[B] = {};
  for (int k = ks * 64; k < ks * 64 + 64; ++k) { float a = bf2f(wg[(size_t)k * F + f]), bb = bf2f(wu[(size_t)k * F + f]);
    for (int b = 0; b < B; ++b) { g[b] += xn[b * D + k] * a; u[b] += xn[b * D + k] * bb; } }
  for (int b = 0; b < B; ++b) { red[0][ks][b][c] = g[b]; red[1][ks][b][c] = u[b]; }
  __syncthreads();
  { int b = tid >> 5; float gg = 0, uu = 0; for (int j = 0; j < 8; ++j) { gg += red[0][j][b][c]; uu += red[1][j][b][c]; }
    hh[b][c] = rb(rb(uu) * rb(1.f / (1.f + __expf(-rb(gg))))); }
  __syncthreads();
  float a0[B] = {}, a1[B] = {};
  for (int j = 0; j < 32; ++j) { u32 w = *(const u32*)(wd + (size_t)(blockIdx.x * 32 + j) * D + 2 * tid);
    for (int b = 0; b < B; ++b) { a0[b] += hh[b][j] * bflo(w); a1[b] += hh[b][j] * bfhi(w); } }
  for (int b = 0; b < B; ++b) { zp[(blockIdx.x * B + b) * D + 2 * tid] = a0[b]; zp[(blockIdx.x * B + b) * D + 2 * tid + 1] = a1[b]; }
}

extern "C" void kernel_launch(void* const* d_in, const int* in_sizes, int n_in, void* d_out, int out_size, void* d_ws, size_t ws_size, hipStream_t stream) {
  const u16* x = (const u16*)d_in[0]; const u16* K = (const u16*)d_in[1]; const u16* V = (const u16*)d_in[2];
  const u16* wq = (const u16*)d_in[3]; const u16* wo = (const u16*)d_in[4];
  const u16* wg = (const u16*)d_in[6]; const u16* wu = (const u16*)d_in[7]; const u16* wd = (const u16*)d_in[8];
  char* ws = (char*)d_ws;
  float* xf = (float*)ws; u16* q = (u16*)(ws + 65536); float* pm = (float*)(ws + 2 * 65536 * 2); float* po = (float*)(ws + (1 << 20)); float* part = (float*)(ws + (4 << 20));
  k_init<<<16, 256, 0, stream>>>(x, xf);
  for (int l = 0; l < 2; ++l) {
    k_q<<<64, 256, 0, stream>>>(xf, wq, q);
    k_attn<<<dim3(NSPLIT, H, B), 256, 0, stream>>>(q, K, V, pm, po);
    k_wo<<<32, 256, 0, stream>>>(pm, po, wo, part);
    k_sum<<<16, 256, 0, stream>>>(part, 32, xf, nullptr);
    k_ffn<<<43, 256, 0, stream>>>(xf, wg, wu, wd, part);
    k_sum<<<16, 256, 0, stream>>>(part, 43, xf, l == 1 ? (u16*)d_out : nullptr);
  }
}
```

```cpp
#include <hip/hip_runtime.h>
#include <stdint.h>

typedef unsigned int u32;
typedef unsigned short u16;

constexpr int NH = 32, SK = 4096, DH = 128;
constexpr int NCHAIN = 32, NSPLIT = 7, NFFN = 22;
constexpr u32 OREC = 384;
constexpr u32 WS_Q = 0;
constexpr u32 WS_Y = WS_Q + 16 * NH * 256;
constexpr u32 WS_Z = WS_Y + 16 * 8 * 1024;
constexpr u32 WS_O = WS_Z + 16 * NFFN * 1024;
constexpr u32 WS_F = WS_O + 4 * NH * NSPLIT * OREC;
constexpr u32 WS_N = WS_F + 32 * 128;

struct Args { const u16* x; const u16* k; const u16* v; const u16* wq; const u16* wo; const u16* wg; const u16* wu; const u16* wd; u16* out; unsigned char* ws; };

#define ALL_REGS "v0", "v1", "v2", "v3", "v4", "v5", "v6", "v7", "v8", "v9", "v10", "v11", "v12", "v13", "v14", "v15", "v16", "v17", "v18", "v19", "v20", "v21", "v22", "v23", "v24", "v25", "v26", "v27", "v28", "v29", "v30", "v31", "v32", "v33", "v34", "v35", "v36", "v37", "v38", "v39", "v40", "v41", "v42", "v43", "v44", "v45", "v46", "v47", "v48", "v49", "v50", "v51", "v52", "v53", "v54", "v55", "v56", "v57", "v58", "v59", "v60", "v61", "v62", "v63", "v64", "v65", "v66", "v67", "v68", "v69", "v70", "v71", "v72", "v73", "v74", "v75", "v76", "v77", "v78", "v79", "v80", "v81", "v82", "v83", "v84", "v85", "v86", "v87", "v88", "v89", "v90", "v91", "v92", "v93", "v94", "v95", "v96", "v97", "v98", "v99", "v100", "v101", "v102", "v103", "v104", "v105", "v106", "v107", "v108", "v109", "v110", "v111", "v112", "v113", "v114", "v115", "v116", "v117", "v118", "v119", "v120", "v121", "v122", "v123", "v124", "v125", "v126", "v127", "v128", "v129", "v130", "v131", "v132", "v133", "v134", "v135", "v136", "v137", "v138", "v139", "v140", "v141", "v142", "v143", "v144", "v145", "v146", "v147", "v148", "v149", "v150", "v151", "v152", "v153", "v154", "v155", "v156", "v157", "v158", "v159", "v160", "v161", "v162", "v163", "v164", "v165", "v166", "v167", "v168", "v169", "v170", "v171", "v172", "v173", "v174", "v175", "v176", "v177", "v178", "v179", "v180", "v181", "v182", "v183", "v184", "v185", "v186", "v187", "v188", "v189", "v190", "v191", "v192", "v193", "v194", "v195", "v196", "v197", "v198", "v199", "v200", "v201", "v202", "v203", "v204", "v205", "v206", "v207", "v208", "v209", "v210", "v211", "v212", "v213", "v214", "v215", "v216", "v217", "v218", "v219", "v220", "v221", "v222", "v223", "v224", "v225", "v226", "v227", "v228", "v229", "v230", "v231", "v232", "v233", "v234", "v235", "v236", "v237", "v238", "v239", "v240", "v241", "v242", "v243", "v244", "v245", "v246", "v247", "v248", "v249", "v250", "v251", "v252", "v253", "v254", "v255", "a0", "a1", "a2", "a3", "a4", "a5", "a6", "a7", "a8", "a9", "a10", "a11", "a12", "a13", "a14", "a15", "a16", "a17", "a18", "a19", "a20", "a21", "a22", "a23", "a24", "a25", "a26", "a27", "a28", "a29", "a30", "a31", "a32", "a33", "a34", "a35", "a36", "a37", "a38", "a39", "a40", "a41", "a42", "a43", "a44", "a45", "a46", "a47", "a48", "a49", "a50", "a51", "a52", "a53", "a54", "a55", "a56", "a57", "a58", "a59", "a60", "a61", "a62", "a63", "a64", "a65", "a66", "a67", "a68", "a69", "a70", "a71", "a72", "a73", "a74", "a75", "a76", "a77", "a78", "a79", "a80", "a81", "a82", "a83", "a84", "a85", "a86", "a87", "a88", "a89", "a90", "a91", "a92", "a93", "a94", "a95", "a96", "a97", "a98", "a99", "a100", "a101", "a102", "a103", "a104", "a105", "a106", "a107", "a108", "a109", "a110", "a111", "a112", "a113", "a114", "a115", "a116", "a117", "a118", "a119", "a120", "a121", "a122", "a123", "a124", "a125", "a126", "a127", "a128", "a129", "a130", "a131", "a132", "a133", "a134", "a135", "a136", "a137", "a138", "a139", "a140", "a141", "a142", "a143", "a144", "a145", "a146", "a147", "a148", "a149", "a150", "a151", "a152", "a153", "a154", "a155", "a156", "a157", "a158", "a159", "a160", "a161", "a162", "a163", "a164", "a165", "a166", "a167", "a168", "a169", "a170", "a171", "a172", "a173", "a174", "a175", "a176", "a177", "a178", "a179", "a180", "a181", "a182", "a183", "a184", "a185", "a186", "a187", "a188", "a189", "a190", "a191", "a192", "a193", "a194", "a195", "a196", "a197", "a198", "a199", "a200", "a201", "a202", "a203", "a204", "a205", "a206", "a207", "a208", "a209", "a210", "a211", "a212", "a213", "a214", "a215", "a216", "a217", "a218", "a219", "a220", "a221", "a222", "a223", "a224", "a225", "a226", "a227", "a228", "a229", "a230", "a231", "a232", "a233", "a234", "a235", "a236", "a237", "a238", "a239", "a240", "a241", "a242", "a243", "a244", "a245", "a246", "a247", "a248", "a249", "a250", "a251", "a252", "a253", "a254", "a255", "s40", "s41", "s42", "s43", "s44", "s45", "s46", "s47", "s48", "s49", "s50", "s51", "s52", "s53", "s54", "s55", "s56", "s57", "s58", "s59", "s60", "s61", "s62", "s63", "s64", "s65", "s66", "s67", "s68", "s69", "s70", "s71", "s72", "s73", "s74", "s75", "s76", "s77", "s78", "s79", "s80", "s81", "s82", "s83", "s84", "s85", "s86", "s87", "s88", "s89", "s90", "s91", "s92", "s93", "s94", "s95", "s96", "s97", "s98", "s99", "vcc", "scc", "memory"

__global__ void __launch_bounds__(256, 1) fused_decode(Args a) {
  __shared__ __attribute__((aligned(16))) unsigned char lds[163840];
  const int w = __builtin_amdgcn_readfirstlane(threadIdx.x >> 6);
  asm volatile("" :: "s"((unsigned)(size_t)lds));
  if (blockIdx.x < NCHAIN) {
    const int j = 4 * (blockIdx.x & 7) + (blockIdx.x >> 3);
    const int f = (j & 3) == 1 ? 24 + (j >> 2) : j - ((j + 2) >> 2);
    asm volatile(
    "s_mov_b32 s50, %8\n"
    "s_mov_b32 s51, %9\n"
    "s_mov_b32 s95, %10\n"
    "v_mbcnt_lo_u32_b32 v24, -1, 0\n"
    "v_mbcnt_hi_u32_b32 v24, -1, v24\n"
    "v_lshrrev_b32 v25, 4, v24\n"
    "v_and_b32 v26, 15, v24\n"
    "v_lshlrev_b32 v0, 4, v26\n"
    "v_lshlrev_b32 v27, 4, v24\n"
    "s_mov_b64 s[40:41], %7\n"
    "s_mov_b32 s42, 972032\n"
    "s_mov_b32 s43, 0x00020000\n"
    "v_mov_b32 v56, -1\n"
    "v_mov_b32 v57, -1\n"
    "v_mov_b32 v58, -1\n"
    "v_mov_b32 v59, -1\n"
    "v_lshl_add_u32 v42, v25, 13, v0\n"
    "s_lshl_b32 s56, s51, 15\n"
    "s_lshl_b32 s57, s50, 8\n"
    "s_add_u32 s56, s56, s57\n"
    "s_add_u32 s57, s56, 131072\n"
    "buffer_store_dwordx4 v[56:59], v42, s[40:43], s56 offen sc1\n"
    "buffer_store_dwordx4 v[56:59], v42, s[40:43], s57 offen sc1\n"
    "s_mul_i32 s56, s51, 88\n"
    "s_add_u32 s56, s56, s95\n"
    "s_lshl_b32 s56, s56, 10\n"
    "s_add_u32 s56, s56, 262144\n"
    "s_cmp_ge_u32 s95, 22\n"
    "s_cbranch_scc1 Cpz_%=\n"
    "buffer_store_dwordx4 v[56:59], v27, s[40:43], s56 offen sc1\n"
    "s_add_u32 s56, s56, 22528\n"
    "buffer_store_dwordx4 v[56:59], v27, s[40:43], s56 offen sc1\n"
    "s_add_u32 s56, s56, 22528\n"
    "buffer_store_dwordx4 v[56:59], v27, s[40:43], s56 offen sc1\n"
    "s_add_u32 s56, s56, 22528\n"
    "buffer_store_dwordx4 v[56:59], v27, s[40:43], s56 offen sc1\n"
    "s_add_u32 s56, s56, 22528\n"
    "s_nop 0\n"
    "Cpz_%=:\n"
    "s_lshl_b32 s56, s95, 2\n"
    "s_add_u32 s56, s56, s51\n"
    "s_cmp_lg_u32 s56, 89\n"
    "s_cbranch_scc1 Cpt_%=\n"
    "s_memrealtime s[86:87]\n"
    "s_mov_b32 s56, 970880\n"
    "s_waitcnt lgkmcnt(0)\n"
    "v_mov_b32 v52, s86\n"
    "v_mov_b32 v53, s86\n"
    "v_mov_b32 v54, s86\n"
    "v_mov_b32 v55, s86\n"
    "s_mov_b64 exec, 0xff\n"
    "buffer_store_dwordx4 v[52:55], v0, s[40:43], s56 offen sc1\n"
    "s_mov_b64 exec, -1\n"
    "s_nop 0\n"
    "Cpt_%=:\n"
    "s_mov_b64 s[44:45], %0\n"
    "s_mov_b32 s46, 8192\n"
    "s_mov_b32 s47, 0x00020000\n"
    "s_lshl_b32 s56, s51, 11\n"
    "buffer_load_dwordx4 v[32:35], v27, s[44:47], s56 offen\n"
    "buffer_load_dwordx4 v[36:39], v27, s[44:47], s56 offen offset:1024\n"
    "v_and_b32 v41, 1, v25\n"
    "v_lshlrev_b32 v41, 5, v41\n"
    "v_and_b32 v40, 2, v25\n"
    "v_add_u32 v41, v40, v41\n"
    "s_lshl_b32 s56, s51, 20\n"
    "s_lshl_b32 s57, s50, 8\n"
    "s_add_u32 s56, s56, s57\n"
    "v_lshl_add_u32 v18, v41, 13, v0\n"
    "v_add_u32 v18, s56, v18\n"
    "s_mov_b64 s[66:67], %1\n"
    "s_add_u32 s88, s66, 0\n"
    "s_addc_u32 s89, s67, 0\n"
    "global_load_dwordx4 v[64:67], v18, s[88:89] nt\n"
    "s_add_u32 s88, s66, 8192\n"
    "s_addc_u32 s89, s67, 0\n"
    "global_load_dwordx4 v[72:75], v18, s[88:89] nt\n"
    "s_add_u32 s88, s66, 65536\n"
    "s_addc_u32 s89, s67, 0\n"
    "global_load_dwordx4 v[80:83], v18, s[88:89] nt\n"
    "s_add_u32 s88, s66, 73728\n"
    "s_addc_u32 s89, s67, 0\n"
    "global_load_dwordx4 v[88:91], v18, s[88:89] nt\n"
    "s_add_u32 s88, s66, 131072\n"
    "s_addc_u32 s89, s67, 0\n"
    "global_load_dwordx4 v[96:99], v18, s[88:89] nt\n"
    "s_add_u32 s88, s66, 139264\n"
    "s_addc_u32 s89, s67, 0\n"
    "global_load_dwordx4 v[104:107], v18, s[88:89] nt\n"
    "s_add_u32 s88, s66, 196608\n"
    "s_addc_u32 s89, s67, 0\n"
    "global_load_dwordx4 v[112:115], v18, s[88:89] nt\n"
    "s_add_u32 s88, s66, 204800\n"
    "s_addc_u32 s89, s67, 0\n"
    "global_load_dwordx4 v[120:123], v18, s[88:89] nt\n"
    "s_load_dwordx2 s[92:93], %7, 970752\n"
    "s_and_b32 s56, s50, 28\n"
    "s_add_u32 s56, s56, s51\n"
    "s_lshl_b32 s56, s56, 17\n"
    "s_and_b32 s57, s50, 3\n"
    "s_lshl_b32 s57, s57, 8\n"
    "s_add_u32 s56, s56, s57\n"
    "v_lshl_add_u32 v1, v41, 10, v0\n"
    "v_add_u32 v1, s56, v1\n"
    "s_mov_b64 s[48:49], %6\n"
    "s_mov_b32 s68, 0xffff0000\n"
    "s_mov_b32 s69, 0xff800000\n"
    "s_mov_b32 s70, 0xbfb8aa3b\n"
    "s_mov_b32 s75, 0x3f803f80\n"
    "s_mov_b32 s81, 0x05040100\n"
    "s_mov_b32 s82, 0x07060302\n"
    "s_mov_b32 s55, 524288\n"
    "s_mov_b32 s62, -1\n"
    "s_mov_b32 s63, 0x0000ffff\n"
    "s_lshl_b32 s83, s95, 12\n"
    "s_add_u32 s83, s83, 8388608\n"
    "s_mov_b64 s[76:77], %3\n"
    "s_mov_b64 s[78:79], %4\n"
    "v_mov_b32 v2, s76\n"
    "v_mov_b32 v3, s77\n"
    "v_mov_b32 v42, s78\n"
    "v_mov_b32 v43, s79\n"
    "v_cmp_gt_u32 vcc, 8, v26\n"
    "s_nop 1\n"
    "v_cndmask_b32 v2, v42, v2, vcc\n"
    "v_cndmask_b32 v3, v43, v3, vcc\n"
    "s_lshl_b32 s56, s95, 6\n"
    "v_and_b32 v42, 7, v26\n"
    "v_lshl_add_u32 v42, v42, 3, s56\n"
    "v_min_u32 v42, 1368, v42\n"
    "v_lshlrev_b32 v42, 1, v42\n"
    "s_mul_i32 s57, s51, 352256\n"
    "v_mul_u32_u24 v43, 2752, v41\n"
    "v_add3_u32 v42, v42, v43, s57\n"
    "v_mov_b32 v43, 0\n"
    "v_lshl_add_u64 v[2:3], v[42:43], 0, v[2:3]\n"
    "v_add_u32 v42, s56, v24\n"
    "v_cmp_gt_u32 vcc, 1376, v42\n"
    "s_mov_b64 s[64:65], vcc\n"
    "s_lshl_b32 s57, s51, 8\n"
    "v_lshl_add_u32 v42, v25, 3, s56\n"
    "v_add_u32 v43, 32, v42\n"
    "v_min_u32 v42, 1368, v42\n"
    "v_lshl_add_u32 v15, v42, 10, v0\n"
    "v_add_u32 v15, s57, v15\n"
    "v_min_u32 v43, 1368, v43\n"
    "v_lshl_add_u32 v16, v43, 10, v0\n"
    "v_add_u32 v16, s57, v16\n"
    "v_lshl_add_u32 v7, v25, 2, v0\n"
    "v_add_u32 v7, s57, v7\n"
    "v_and_b32 v42, 1, v26\n"
    "v_lshlrev_b32 v42, 4, v42\n"
    "v_mov_b32 v43, 0xffff\n"
    "v_lshlrev_b32 v43, v42, v43\n"
    "v_lshrrev_b32 v42, 1, v26\n"
    "v_cmp_eq_u32 vcc, 0, v42\n"
    "s_nop 1\n"
    "v_cndmask_b32 v8, 0, v43, vcc\n"
    "v_cmp_eq_u32 vcc, 2, v42\n"
    "s_nop 1\n"
    "v_cndmask_b32 v9, 0, v43, vcc\n"
    "v_cmp_eq_u32 vcc, 4, v42\n"
    "s_nop 1\n"
    "v_cndmask_b32 v10, 0, v43, vcc\n"
    "v_cmp_eq_u32 vcc, 6, v42\n"
    "s_nop 1\n"
    "v_cndmask_b32 v11, 0, v43, vcc\n"
    "s_lshl_b32 s57, s51, 9\n"
    "v_lshlrev_b32 v13, 1, v0\n"
    "v_lshl_add_u32 v12, v25, 4, v13\n"
    "v_add_u32 v12, s57, v12\n"
    "v_lshl_add_u32 v13, v25, 3, v13\n"
    "v_lshlrev_b32 v14, 2, v24\n"
    "v_bfe_u32 v42, v26, 2, 1\n"
    "v_mul_u32_u24 v19, 0xaaaa, v42\n"
    "v_add_u32 v19, 0x4444, v19\n"
    "s_lshl_b32 s60, s51, 3\n"
    "s_waitcnt vmcnt(8)\n"
    "s_lshl_b32 s57, s51, 11\n"
    "v_add_u32 v27, s57, v27\n"
    "v_lshlrev_b32 v44, 16, v32\n"
    "v_and_b32 v45, s68, v32\n"
    "v_lshlrev_b32 v46, 16, v33\n"
    "v_and_b32 v47, s68, v33\n"
    "v_lshlrev_b32 v48, 16, v34\n"
    "v_and_b32 v49, s68, v34\n"
    "v_lshlrev_b32 v50, 16, v35\n"
    "v_and_b32 v51, s68, v35\n"
    "v_add_f32 v52, v44, v45\n"
    "v_add_f32 v52, v52, v46\n"
    "v_add_f32 v52, v52, v47\n"
    "v_add_f32 v52, v52, v48\n"
    "v_add_f32 v52, v52, v49\n"
    "v_add_f32 v52, v52, v50\n"
    "v_add_f32 v52, v52, v51\n"
    "s_waitcnt vmcnt(7)\n"
    "s_mov_b64 s[66:67], %1\n"
    "s_add_u32 s88, s66, 32768\n"
    "s_addc_u32 s89, s67, 0\n"
    "global_load_dwordx4 v[68:71], v18, s[88:89] nt\n"
    "v_add_f32_dpp v52, v52, v52 row_ror:8 row_mask:0xf bank_mask:0xf\n"
    "s_nop 1\n"
    "v_add_f32_dpp v52, v52, v52 row_ror:4 row_mask:0xf bank_mask:0xf\n"
    "s_nop 1\n"
    "v_add_f32_dpp v52, v52, v52 row_ror:2 row_mask:0xf bank_mask:0xf\n"
    "s_nop 1\n"
    "v_add_f32_dpp v52, v52, v52 row_ror:1 row_mask:0xf bank_mask:0xf\n"
    "v_mov_b32 v53, v52\n"
    "s_nop 1\n"
    "v_permlane16_swap_b32 v52, v53\n"
    "v_add_f32 v52, v52, v53\n"
    "v_mov_b32 v53, v52\n"
    "s_nop 1\n"
    "v_permlane32_swap_b32 v52, v53\n"
    "v_add_f32 v52, v52, v53\n"
    "v_mul_f32 v52, 0x3b000000, v52\n"
    "s_waitcnt vmcnt(7)\n"
    "s_add_u32 s88, s66, 40960\n"
    "s_addc_u32 s89, s67, 0\n"
    "global_load_dwordx4 v[76:79], v18, s[88:89] nt\n"
    "v_mov_b32 v54, 0\n"
    "v_sub_f32 v53, v44, v52\n"
    "v_fmac_f32 v54, v53, v53\n"
    "v_sub_f32 v53, v45, v52\n"
    "v_fmac_f32 v54, v53, v53\n"
    "v_sub_f32 v53, v46, v52\n"
    "v_fmac_f32 v54, v53, v53\n"
    "v_sub_f32 v53, v47, v52\n"
    "v_fmac_f32 v54, v53, v53\n"
    "v_sub_f32 v53, v48, v52\n"
    "v_fmac_f32 v54, v53, v53\n"
    "v_sub_f32 v53, v49, v52\n"
    "v_fmac_f32 v54, v53, v53\n"
    "v_sub_f32 v53, v50, v52\n"
    "v_fmac_f32 v54, v53, v53\n"
    "v_sub_f32 v53, v51, v52\n"
    "v_fmac_f32 v54, v53, v53\n"
    "s_waitcnt vmcnt(7)\n"
    "s_add_u32 s88, s66, 98304\n"
    "s_addc_u32 s89, s67, 0\n"
    "global_load_dwordx4 v[84:87], v18, s[88:89] nt\n"
    "v_add_f32_dpp v54, v54, v54 row_ror:8 row_mask:0xf bank_mask:0xf\n"
    "s_nop 1\n"
    "v_add_f32_dpp v54, v54, v54 row_ror:4 row_mask:0xf bank_mask:0xf\n"
    "s_nop 1\n"
    "v_add_f32_dpp v54, v54, v54 row_ror:2 row_mask:0xf bank_mask:0xf\n"
    "s_nop 1\n"
    "v_add_f32_dpp v54, v54, v54 row_ror:1 row_mask:0xf bank_mask:0xf\n"
    "v_mov_b32 v53, v54\n"
    "s_nop 1\n"
    "v_permlane16_swap_b32 v54, v53\n"
    "v_add_f32 v54, v54, v53\n"
    "v_mov_b32 v53, v54\n"
    "s_nop 1\n"
    "v_permlane32_swap_b32 v54, v53\n"
    "v_add_f32 v54, v54, v53\n"
    "v_mul_f32 v54, 0x3b000000, v54\n"
    "s_waitcnt vmcnt(7)\n"
    "s_add_u32 s88, s66, 106496\n"
    "s_addc_u32 s89, s67, 0\n"
    "global_load_dwordx4 v[92:95], v18, s[88:89] nt\n"
    "v_cvt_pk_bf16_f32 v52, v52, v52\n"
    "v_and_b32 v52, s68, v52\n"
    "v_cvt_pk_bf16_f32 v54, v54, v54\n"
    "v_and_b32 v54, s68, v54\n"
    "v_add_f32 v54, 0x3727c5ac, v54\n"
    "v_cvt_pk_bf16_f32 v54, v54, v54\n"
    "v_and_b32 v54, s68, v54\n"
    "v_sqrt_f32 v54, v54\n"
    "s_nop 0\n"
    "v_cvt_pk_bf16_f32 v54, v54, v54\n"
    "v_and_b32 v54, s68, v54\n"
    "v_rcp_f32 v54, v54\n"
    "v_sub_f32 v44, v44, v52\n"
    "v_sub_f32 v45, v45, v52\n"
    "v_sub_f32 v46, v46, v52\n"
    "v_sub_f32 v47, v47, v52\n"
    "v_sub_f32 v48, v48, v52\n"
    "v_sub_f32 v49, v49, v52\n"
    "v_sub_f32 v50, v50, v52\n"
    "v_sub_f32 v51, v51, v52\n"
    "v_cvt_pk_bf16_f32 v53, v44, v45\n"
    "v_lshlrev_b32 v44, 16, v53\n"
    "v_and_b32 v45, s68, v53\n"
    "v_mul_f32 v44, v54, v44\n"
    "v_mul_f32 v45, v54, v45\n"
    "v_cvt_pk_bf16_f32 v32, v44, v45\n"
    "v_cvt_pk_bf16_f32 v53, v46, v47\n"
    "v_lshlrev_b32 v46, 16, v53\n"
    "v_and_b32 v47, s68, v53\n"
    "v_mul_f32 v46, v54, v46\n"
    "v_mul_f32 v47, v54, v47\n"
    "v_cvt_pk_bf16_f32 v33, v46, v47\n"
    "v_cvt_pk_bf16_f32 v53, v48, v49\n"
    "v_lshlrev_b32 v48, 16, v53\n"
    "v_and_b32 v49, s68, v53\n"
    "v_mul_f32 v48, v54, v48\n"
    "v_mul_f32 v49, v54, v49\n"
    "v_cvt_pk_bf16_f32 v34, v48, v49\n"
    "v_cvt_pk_bf16_f32 v53, v50, v51\n"
    "v_lshlrev_b32 v50, 16, v53\n"
    "v_and_b32 v51, s68, v53\n"
    "v_mul_f32 v50, v54, v50\n"
    "v_mul_f32 v51, v54, v51\n"
    "v_cvt_pk_bf16_f32 v35, v50, v51\n"
    "ds_write_b128 v27, v[32:35] offset:16384\n"
    "v_lshlrev_b32 v44, 16, v36\n"
    "v_and_b32 v45, s68, v36\n"
    "v_lshlrev_b32 v46, 16, v37\n"
    "v_and_b32 v47, s68, v37\n"
    "v_lshlrev_b32 v48, 16, v38\n"
    "v_and_b32 v49, s68, v38\n"
    "v_lshlrev_b32 v50, 16, v39\n"
    "v_and_b32 v51, s68, v39\n"
    "v_add_f32 v52, v44, v45\n"
    "v_add_f32 v52, v52, v46\n"
    "v_add_f32 v52, v52, v47\n"
    "v_add_f32 v52, v52, v48\n"
    "v_add_f32 v52, v52, v49\n"
    "v_add_f32 v52, v52, v50\n"
    "v_add_f32 v52, v52, v51\n"
    "s_waitcnt vmcnt(7)\n"
    "s_add_u32 s88, s66, 163840\n"
    "s_addc_u32 s89, s67, 0\n"
    "global_load_dwordx4 v[100:103], v18, s[88:89] nt\n"
    "v_add_f32_dpp v52, v52, v52 row_ror:8 row_mask:0xf bank_mask:0xf\n"
    "s_nop 1\n"
    "v_add_f32_dpp v52, v52, v52 row_ror:4 row_mask:0xf bank_mask:0xf\n"
    "s_nop 1\n"
    "v_add_f32_dpp v52, v52, v52 row_ror:2 row_mask:0xf bank_mask:0xf\n"
    "s_nop 1\n"
    "v_add_f32_dpp v52, v52, v52 row_ror:1 row_mask:0xf bank_mask:0xf\n"
    "v_mov_b32 v53, v52\n"
    "s_nop 1\n"
    "v_permlane16_swap_b32 v52, v53\n"
    "v_add_f32 v52, v52, v53\n"
    "v_mov_b32 v53, v52\n"
    "s_nop 1\n"
    "v_permlane32_swap_b32 v52, v53\n"
    "v_add_f32 v52, v52, v53\n"
    "v_mul_f32 v52, 0x3b000000, v52\n"
    "s_waitcnt vmcnt(7)\n"
    "s_add_u32 s88, s66, 172032\n"
    "s_addc_u32 s89, s67, 0\n"
    "global_load_dwordx4 v[108:111], v18, s[88:89] nt\n"
    "v_mov_b32 v54, 0\n"
    "v_sub_f32 v53, v44, v52\n"
    "v_fmac_f32 v54, v53, v53\n"
    "v_sub_f32 v53, v45, v52\n"
    "v_fmac_f32 v54, v53, v53\n"
    "v_sub_f32 v53, v46, v52\n"
    "v_fmac_f32 v54, v53, v53\n"
    "v_sub_f32 v53, v47, v52\n"
    "v_fmac_f32 v54, v53, v53\n"
    "v_sub_f32 v53, v48, v52\n"
    "v_fmac_f32 v54, v53, v53\n"
    "v_sub_f32 v53, v49, v52\n"
    "v_fmac_f32 v54, v53, v53\n"
    "v_sub_f32 v53, v50, v52\n"
    "v_fmac_f32 v54, v53, v53\n"
    "v_sub_f32 v53, v51, v52\n"
    "v_fmac_f32 v54, v53, v53\n"
    "s_waitcnt vmcnt(7)\n"
    "s_add_u32 s88, s66, 229376\n"
    "s_addc_u32 s89, s67, 0\n"
    "global_load_dwordx4 v[116:119], v18, s[88:89] nt\n"
    "v_add_f32_dpp v54, v54, v54 row_ror:8 row_mask:0xf bank_mask:0xf\n"
    "s_nop 1\n"
    "v_add_f32_dpp v54, v54, v54 row_ror:4 row_mask:0xf bank_mask:0xf\n"
    "s_nop 1\n"
    "v_add_f32_dpp v54, v54, v54 row_ror:2 row_mask:0xf bank_mask:0xf\n"
    "s_nop 1\n"
    "v_add_f32_dpp v54, v54, v54 row_ror:1 row_mask:0xf bank_mask:0xf\n"
    "v_mov_b32 v53, v54\n"
    "s_nop 1\n"
    "v_permlane16_swap_b32 v54, v53\n"
    "v_add_f32 v54, v54, v53\n"
    "v_mov_b32 v53, v54\n"
    "s_nop 1\n"
    "v_permlane32_swap_b32 v54, v53\n"
    "v_add_f32 v54, v54, v53\n"
    "v_mul_f32 v54, 0x3b000000, v54\n"
    "s_waitcnt vmcnt(7)\n"
    "s_add_u32 s88, s66, 237568\n"
    "s_addc_u32 s89, s67, 0\n"
    "global_load_dwordx4 v[124:127], v18, s[88:89] nt\n"
    "v_cvt_pk_bf16_f32 v52, v52, v52\n"
    "v_and_b32 v52, s68, v52\n"
    "v_cvt_pk_bf16_f32 v54, v54, v54\n"
    "v_and_b32 v54, s68, v54\n"
    "v_add_f32 v54, 0x3727c5ac, v54\n"
    "v_cvt_pk_bf16_f32 v54, v54, v54\n"
    "v_and_b32 v54, s68, v54\n"
    "v_sqrt_f32 v54, v54\n"
    "s_nop 0\n"
    "v_cvt_pk_bf16_f32 v54, v54, v54\n"
    "v_and_b32 v54, s68, v54\n"
    "v_rcp_f32 v54, v54\n"
    "v_sub_f32 v44, v44, v52\n"
    "v_sub_f32 v45, v45, v52\n"
    "v_sub_f32 v46, v46, v52\n"
    "v_sub_f32 v47, v47, v52\n"
    "v_sub_f32 v48, v48, v52\n"
    "v_sub_f32 v49, v49, v52\n"
    "v_sub_f32 v50, v50, v52\n"
    "v_sub_f32 v51, v51, v52\n"
    "v_cvt_pk_bf16_f32 v53, v44, v45\n"
    "v_lshlrev_b32 v44, 16, v53\n"
    "v_and_b32 v45, s68, v53\n"
    "v_mul_f32 v44, v54, v44\n"
    "v_mul_f32 v45, v54, v45\n"
    "v_cvt_pk_bf16_f32 v36, v44, v45\n"
    "v_cvt_pk_bf16_f32 v53, v46, v47\n"
    "v_lshlrev_b32 v46, 16, v53\n"
    "v_and_b32 v47, s68, v53\n"
    "v_mul_f32 v46, v54, v46\n"
    "v_mul_f32 v47, v54, v47\n"
    "v_cvt_pk_bf16_f32 v37, v46, v47\n"
    "v_cvt_pk_bf16_f32 v53, v48, v49\n"
    "v_lshlrev_b32 v48, 16, v53\n"
    "v_and_b32 v49, s68, v53\n"
    "v_mul_f32 v48, v54, v48\n"
    "v_mul_f32 v49, v54, v49\n"
    "v_cvt_pk_bf16_f32 v38, v48, v49\n"
    "v_cvt_pk_bf16_f32 v53, v50, v51\n"
    "v_lshlrev_b32 v50, 16, v53\n"
    "v_and_b32 v51, s68, v53\n"
    "v_mul_f32 v50, v54, v50\n"
    "v_mul_f32 v51, v54, v51\n"
    "v_cvt_pk_bf16_f32 v39, v50, v51\n"
    "ds_write_b128 v27, v[36:39] offset:17408\n"
    "v_and_b32 v27, 7, v26\n"
    "v_lshlrev_b32 v27, 10, v27\n"
    "v_lshl_add_u32 v27, v41, 1, v27\n"
    "s_lshl_b32 s57, s51, 8\n"
    "v_add_u32 v27, s57, v27\n"
    "s_waitcnt lgkmcnt(0)\n"
    "s_barrier\n"
    "s_add_u32 s92, s92, 1\n"
    "s_addc_u32 s93, s93, 0\n"
    "s_cmp_lg_u32 s51, 3\n"
    "s_cbranch_scc1 Cpf_%=\n"
    "v_mov_b32 v56, s92\n"
    "v_mov_b32 v57, s93\n"
    "v_mov_b32 v58, s92\n"
    "v_mov_b32 v59, s93\n"
    "s_lshl_b32 s56, s50, 7\n"
    "s_add_u32 s56, s56, 966656\n"
    "s_mov_b64 exec, 0xff\n"
    "buffer_store_dwordx4 v[56:59], v0, s[40:43], s56 offen sc1\n"
    "s_mov_b64 exec, -1\n"
    "s_nop 0\n"
    "Cpf_%=:\n"
    "ds_read_b32 v28, v27 offset:16384\n"
    "ds_read_b32 v29, v27 offset:16400\n"
    "ds_read_b32 v30, v27 offset:16416\n"
    "ds_read_b32 v31, v27 offset:16432\n"
    "s_waitcnt vmcnt(8)\n"
    "s_waitcnt vmcnt(7)\n"
    "s_mov_b64 s[66:67], %1\n"
    "s_add_u32 s88, s66, 524288\n"
    "s_addc_u32 s89, s67, 0\n"
    "global_load_dwordx4 v[128:131], v18, s[88:89] nt\n"
    "v_perm_b32 v20, v72, v64, s81\n"
    "v_perm_b32 v21, v88, v80, s81\n"
    "v_perm_b32 v22, v104, v96, s81\n"
    "v_perm_b32 v23, v120, v112, s81\n"
    "v_accvgpr_write_b32 a128, v20\n"
    "v_accvgpr_write_b32 a129, v21\n"
    "v_accvgpr_write_b32 a130, v22\n"
    "v_accvgpr_write_b32 a131, v23\n"
    "s_waitcnt vmcnt(7)\n"
    "s_add_u32 s88, s66, 532480\n"
    "s_addc_u32 s89, s67, 0\n"
    "global_load_dwordx4 v[136:139], v18, s[88:89] nt\n"
    "v_perm_b32 v20, v72, v64, s82\n"
    "v_perm_b32 v21, v88, v80, s82\n"
    "v_perm_b32 v22, v104, v96, s82\n"
    "v_perm_b32 v23, v120, v112, s82\n"
    "v_accvgpr_write_b32 a136, v20\n"
    "v_accvgpr_write_b32 a137, v21\n"
    "v_accvgpr_write_b32 a138, v22\n"
    "v_accvgpr_write_b32 a139, v23\n"
    "s_waitcnt vmcnt(7)\n"
    "s_add_u32 s88, s66, 589824\n"
    "s_addc_u32 s89, s67, 0\n"
    "global_load_dwordx4 v[144:147], v18, s[88:89] nt\n"
    "v_perm_b32 v20, v73, v65, s81\n"
    "v_perm_b32 v21, v89, v81, s81\n"
    "v_perm_b32 v22, v105, v97, s81\n"
    "v_perm_b32 v23, v121, v113, s81\n"
    "v_accvgpr_write_b32 a144, v20\n"
    "v_accvgpr_write_b32 a145, v21\n"
    "v_accvgpr_write_b32 a146, v22\n"
    "v_accvgpr_write_b32 a147, v23\n"
    "s_waitcnt vmcnt(7)\n"
    "s_add_u32 s88, s66, 598016\n"
    "s_addc_u32 s89, s67, 0\n"
    "global_load_dwordx4 v[152:155], v18, s[88:89] nt\n"
    "v_perm_b32 v20, v73, v65, s82\n"
    "v_perm_b32 v21, v89, v81, s82\n"
    "v_perm_b32 v22, v105, v97, s82\n"
    "v_perm_b32 v23, v121, v113, s82\n"
    "v_accvgpr_write_b32 a152, v20\n"
    "v_accvgpr_write_b32 a153, v21\n"
    "v_accvgpr_write_b32 a154, v22\n"
    "v_accvgpr_write_b32 a155, v23\n"
    "s_waitcnt vmcnt(7)\n"
    "s_add_u32 s88, s66, 655360\n"
    "s_addc_u32 s89, s67, 0\n"
    "global_load_dwordx4 v[160:163], v18, s[88:89] nt\n"
    "v_perm_b32 v20, v74, v66, s81\n"
    "v_perm_b32 v21, v90, v82, s81\n"
    "v_perm_b32 v22, v106, v98, s81\n"
    "v_perm_b32 v23, v122, v114, s81\n"
    "v_accvgpr_write_b32 a160, v20\n"
    "v_accvgpr_write_b32 a161, v21\n"
    "v_accvgpr_write_b32 a162, v22\n"
    "v_accvgpr_write_b32 a163, v23\n"
    "s_waitcnt vmcnt(7)\n"
    "s_add_u32 s88, s66, 663552\n"
    "s_addc_u32 s89, s67, 0\n"
    "global_load_dwordx4 v[168:171], v18, s[88:89] nt\n"
    "v_perm_b32 v20, v74, v66, s82\n"
    "v_perm_b32 v21, v90, v82, s82\n"
    "v_perm_b32 v22, v106, v98, s82\n"
    "v_perm_b32 v23, v122, v114, s82\n"
    "v_accvgpr_write_b32 a168, v20\n"
    "v_accvgpr_write_b32 a169, v21\n"
    "v_accvgpr_write_b32 a170, v22\n"
    "v_accvgpr_write_b32 a171, v23\n"
    "s_waitcnt vmcnt(7)\n"
    "s_add_u32 s88, s66, 720896\n"
    "s_addc_u32 s89, s67, 0\n"
    "global_load_dwordx4 v[176:179], v18, s[88:89] nt\n"
    "v_perm_b32 v20, v75, v67, s81\n"
    "v_perm_b32 v21, v91, v83, s81\n"
    "v_perm_b32 v22, v107, v99, s81\n"
    "v_perm_b32 v23, v123, v115, s81\n"
    "v_accvgpr_write_b32 a176, v20\n"
    "v_accvgpr_write_b32 a177, v21\n"
    "v_accvgpr_write_b32 a178, v22\n"
    "v_accvgpr_write_b32 a179, v23\n"
    "s_waitcnt vmcnt(7)\n"
    "s_add_u32 s88, s66, 729088\n"
    "s_addc_u32 s89, s67, 0\n"
    "global_load_dwordx4 v[184:187], v18, s[88:89] nt\n"
    "v_perm_b32 v20, v75, v67, s82\n"
    "v_perm_b32 v21, v91, v83, s82\n"
    "v_perm_b32 v22, v107, v99, s82\n"
    "v_perm_b32 v23, v123, v115, s82\n"
    "v_accvgpr_write_b32 a184, v20\n"
    "v_accvgpr_write_b32 a185, v21\n"
    "v_accvgpr_write_b32 a186, v22\n"
    "v_accvgpr_write_b32 a187, v23\n"
    "s_waitcnt lgkmcnt(0)\n"
    "v_mfma_f32_16x16x32_bf16 v[32:35], v[28:31], a[128:131], 0\n"
    "v_mfma_f32_16x16x32_bf16 v[36:39], v[28:31], a[136:139], 0\n"
    "v_mfma_f32_16x16x32_bf16 v[40:43], v[28:31], a[144:147], 0\n"
    "v_mfma_f32_16x16x32_bf16 v[44:47], v[28:31], a[152:155], 0\n"
    "v_mfma_f32_16x16x32_bf16 v[48:51], v[28:31], a[160:163], 0\n"
    "v_mfma_f32_16x16x32_bf16 v[52:55], v[28:31], a[168:171], 0\n"
    "v_mfma_f32_16x16x32_bf16 v[56:59], v[28:31], a[176:179], 0\n"
    "v_mfma_f32_16x16x32_bf16 v[60:63], v[28:31], a[184:187], 0\n"
    "ds_read_b32 v28, v27 offset:16392\n"
    "ds_read_b32 v29, v27 offset:16408\n"
    "ds_read_b32 v30, v27 offset:16424\n"
    "ds_read_b32 v31, v27 offset:16440\n"
    "s_waitcnt vmcnt(8)\n"
    "s_waitcnt vmcnt(7)\n"
    "s_mov_b64 s[66:67], %1\n"
    "s_add_u32 s88, s66, 557056\n"
    "s_addc_u32 s89, s67, 0\n"
    "global_load_dwordx4 v[132:135], v18, s[88:89] nt\n"
    "v_perm_b32 v20, v76, v68, s81\n"
    "v_perm_b32 v21, v92, v84, s81\n"
    "v_perm_b32 v22, v108, v100, s81\n"
    "v_perm_b32 v23, v124, v116, s81\n"
    "v_accvgpr_write_b32 a132, v20\n"
    "v_accvgpr_write_b32 a133, v21\n"
    "v_accvgpr_write_b32 a134, v22\n"
    "v_accvgpr_write_b32 a135, v23\n"
    "s_waitcnt vmcnt(7)\n"
    "s_add_u32 s88, s66, 565248\n"
    "s_addc_u32 s89, s67, 0\n"
    "global_load_dwordx4 v[140:143], v18, s[88:89] nt\n"
    "v_perm_b32 v20, v76, v68, s82\n"
    "v_perm_b32 v21, v92, v84, s82\n"
    "v_perm_b32 v22, v108, v100, s82\n"
    "v_perm_b32 v23, v124, v116, s82\n"
    "v_accvgpr_write_b32 a140, v20\n"
    "v_accvgpr_write_b32 a141, v21\n"
    "v_accvgpr_write_b32 a142, v22\n"
    "v_accvgpr_write_b32 a143, v23\n"
    "s_waitcnt vmcnt(7)\n"
    "s_add_u32 s88, s66, 622592\n"
    "s_addc_u32 s89, s67, 0\n"
    "global_load_dwordx4 v[148:151], v18, s[88:89] nt\n"
    "v_perm_b32 v20, v77, v69, s81\n"
    "v_perm_b32 v21, v93, v85, s81\n"
    "v_perm_b32 v22, v109, v101, s81\n"
    "v_perm_b32 v23, v125, v117, s81\n"
    "v_accvgpr_write_b32 a148, v20\n"
    "v_accvgpr_write_b32 a149, v21\n"
    "v_accvgpr_write_b32 a150, v22\n"
    "v_accvgpr_write_b32 a151, v23\n"
    "s_waitcnt vmcnt(7)\n"
    "s_add_u32 s88, s66, 630784\n"
    "s_addc_u32 s89, s67, 0\n"
    "global_load_dwordx4 v[156:159], v18, s[88:89] nt\n"
    "v_perm_b32 v20, v77, v69, s82\n"
    "v_perm_b32 v21, v93, v85, s82\n"
    "v_perm_b32 v22, v109, v101, s82\n"
    "v_perm_b32 v23, v125, v117, s82\n"
    "v_accvgpr_write_b32 a156, v20\n"
    "v_accvgpr_write_b32 a157, v21\n"
    "v_accvgpr_write_b32 a158, v22\n"
    "v_accvgpr_write_b32 a159, v23\n"
    "s_waitcnt vmcnt(7)\n"
    "s_add_u32 s88, s66, 688128\n"
    "s_addc_u32 s89, s67, 0\n"
    "global_load_dwordx4 v[164:167], v18, s[88:89] nt\n"
    "v_perm_b32 v20, v78, v70, s81\n"
    "v_perm_b32 v21, v94, v86, s81\n"
    "v_perm_b32 v22, v110, v102, s81\n"
    "v_perm_b32 v23, v126, v118, s81\n"
    "v_accvgpr_write_b32 a164, v20\n"
    "v_accvgpr_write_b32 a165, v21\n"
    "v_accvgpr_write_b32 a166, v22\n"
    "v_accvgpr_write_b32 a167, v23\n"
    "s_waitcnt vmcnt(7)\n"
    "s_add_u32 s88, s66, 696320\n"
    "s_addc_u32 s89, s67, 0\n"
    "global_load_dwordx4 v[172:175], v18, s[88:89] nt\n"
    "v_perm_b32 v20, v78, v70, s82\n"
    "v_perm_b32 v21, v94, v86, s82\n"
    "v_perm_b32 v22, v110, v102, s82\n"
    "v_perm_b32 v23, v126, v118, s82\n"
    "v_accvgpr_write_b32 a172, v20\n"
    "v_accvgpr_write_b32 a173, v21\n"
    "v_accvgpr_write_b32 a174, v22\n"
    "v_accvgpr_write_b32 a175, v23\n"
    "s_waitcnt vmcnt(7)\n"
    "s_add_u32 s88, s66, 753664\n"
    "s_addc_u32 s89, s67, 0\n"
    "global_load_dwordx4 v[180:183], v18, s[88:89] nt\n"
    "v_perm_b32 v20, v79, v71, s81\n"
    "v_perm_b32 v21, v95, v87, s81\n"
    "v_perm_b32 v22, v111, v103, s81\n"
    "v_perm_b32 v23, v127, v119, s81\n"
    "v_accvgpr_write_b32 a180, v20\n"
    "v_accvgpr_write_b32 a181, v21\n"
    "v_accvgpr_write_b32 a182, v22\n"
    "v_accvgpr_write_b32 a183, v23\n"
    "s_waitcnt vmcnt(7)\n"
    "s_add_u32 s88, s66, 761856\n"
    "s_addc_u32 s89, s67, 0\n"
    "global_load_dwordx4 v[188:191], v18, s[88:89] nt\n"
    "v_perm_b32 v20, v79, v71, s82\n"
    "v_perm_b32 v21, v95, v87, s82\n"
    "v_perm_b32 v22, v111, v103, s82\n"
    "v_perm_b32 v23, v127, v119, s82\n"
    "v_accvgpr_write_b32 a188, v20\n"
    "v_accvgpr_write_b32 a189, v21\n"
    "v_accvgpr_write_b32 a190, v22\n"
    "v_accvgpr_write_b32 a191, v23\n"
    "s_waitcnt lgkmcnt(0)\n"
    "v_mfma_f32_16x16x32_bf16 v[32:35], v[28:31], a[132:135], v[32:35]\n"
    "v_mfma_f32_16x16x32_bf16 v[36:39], v[28:31], a[140:143], v[36:39]\n"
    "v_mfma_f32_16x16x32_bf16 v[40:43], v[28:31], a[148:151], v[40:43]\n"
    "v_mfma_f32_16x16x32_bf16 v[44:47], v[28:31], a[156:159], v[44:47]\n"
    "v_mfma_f32_16x16x32_bf16 v[48:51], v[28:31], a[164:167], v[48:51]\n"
    "v_mfma_f32_16x16x32_bf16 v[52:55], v[28:31], a[172:175], v[52:55]\n"
    "v_mfma_f32_16x16x32_bf16 v[56:59], v[28:31], a[180:183], v[56:59]\n"
    "v_mfma_f32_16x16x32_bf16 v[60:63], v[28:31], a[188:191], v[60:63]\n"
    "ds_read_b32 v28, v27 offset:16512\n"
    "ds_read_b32 v29, v27 offset:16528\n"
    "ds_read_b32 v30, v27 offset:16544\n"
    "ds_read_b32 v31, v27 offset:16560\n"
    "s_waitcnt vmcnt(8)\n"
    "s_waitcnt vmcnt(7)\n"
    "s_mov_b64 s[66:67], %2\n"
    "s_add_u32 s88, s66, 0\n"
    "s_addc_u32 s89, s67, 0\n"
    "global_load_dwordx4 v[192:195], v1, s[88:89] nt\n"
    "v_perm_b32 v20, v136, v128, s81\n"
    "v_perm_b32 v21, v152, v144, s81\n"
    "v_perm_b32 v22, v168, v160, s81\n"
    "v_perm_b32 v23, v184, v176, s81\n"
    "v_accvgpr_write_b32 a192, v20\n"
    "v_accvgpr_write_b32 a193, v21\n"
    "v_accvgpr_write_b32 a194, v22\n"
    "v_accvgpr_write_b32 a195, v23\n"
    "s_waitcnt vmcnt(7)\n"
    "s_add_u32 s88, s66, 1024\n"
    "s_addc_u32 s89, s67, 0\n"
    "global_load_dwordx4 v[200:203], v1, s[88:89] nt\n"
    "v_perm_b32 v20, v136, v128, s82\n"
    "v_perm_b32 v21, v152, v144, s82\n"
    "v_perm_b32 v22, v168, v160, s82\n"
    "v_perm_b32 v23, v184, v176, s82\n"
    "v_accvgpr_write_b32 a200, v20\n"
    "v_accvgpr_write_b32 a201, v21\n"
    "v_accvgpr_write_b32 a202, v22\n"
    "v_accvgpr_write_b32 a203, v23\n"
    "s_waitcnt vmcnt(7)\n"
    "s_add_u32 s88, s66, 8192\n"
    "s_addc_u32 s89, s67, 0\n"
    "global_load_dwordx4 v[208:211], v1, s[88:89] nt\n"
    "v_perm_b32 v20, v137, v129, s81\n"
    "v_perm_b32 v21, v153, v145, s81\n"
    "v_perm_b32 v22, v169, v161, s81\n"
    "v_perm_b32 v23, v185, v177, s81\n"
    "v_accvgpr_write_b32 a208, v20\n"
    "v_accvgpr_write_b32 a209, v21\n"
    "v_accvgpr_write_b32 a210, v22\n"
    "v_accvgpr_write_b32 a211, v23\n"
    "s_waitcnt vmcnt(7)\n"
    "s_add_u32 s88, s66, 9216\n"
    "s_addc_u32 s89, s67, 0\n"
    "global_load_dwordx4 v[216:219], v1, s[88:89] nt\n"
    "v_perm_b32 v20, v137, v129, s82\n"
    "v_perm_b32 v21, v153, v145, s82\n"
    "v_perm_b32 v22, v169, v161, s82\n"
    "v_perm_b32 v23, v185, v177, s82\n"
    "v_accvgpr_write_b32 a216, v20\n"
    "v_accvgpr_write_b32 a217, v21\n"
    "v_accvgpr_write_b32 a218, v22\n"
    "v_accvgpr_write_b32 a219, v23\n"
    "s_waitcnt vmcnt(7)\n"
    "s_add_u32 s88, s66, 16384\n"
    "s_addc_u32 s89, s67, 0\n"
    "global_load_dwordx4 v[224:227], v1, s[88:89] nt\n"
    "v_perm_b32 v20, v138, v130, s81\n"
    "v_perm_b32 v21, v154, v146, s81\n"
    "v_perm_b32 v22, v170, v162, s81\n"
    "v_perm_b32 v23, v186, v178, s81\n"
    "v_accvgpr_write_b32 a224, v20\n"
    "v_accvgpr_write_b32 a225, v21\n"
    "v_accvgpr_write_b32 a226, v22\n"
    "v_accvgpr_write_b32 a227, v23\n"
    "s_waitcnt vmcnt(7)\n"
    "s_add_u32 s88, s66, 17408\n"
    "s_addc_u32 s89, s67, 0\n"
    "global_load_dwordx4 v[232:235], v1, s[88:89] nt\n"
    "v_perm_b32 v20, v138, v130, s82\n"
    "v_perm_b32 v21, v154, v146, s82\n"
    "v_perm_b32 v22, v170, v162, s82\n"
    "v_perm_b32 v23, v186, v178, s82\n"
    "v_accvgpr_write_b32 a232, v20\n"
    "v_accvgpr_write_b32 a233, v21\n"
    "v_accvgpr_write_b32 a234, v22\n"
    "v_accvgpr_write_b32 a235, v23\n"
    "s_waitcnt vmcnt(7)\n"
    "s_add_u32 s88, s66, 24576\n"
    "s_addc_u32 s89, s67, 0\n"
    "global_load_dwordx4 v[240:243], v1, s[88:89] nt\n"
    "v_perm_b32 v20, v139, v131, s81\n"
    "v_perm_b32 v21, v155, v147, s81\n"
    "v_perm_b32 v22, v171, v163, s81\n"
    "v_perm_b32 v23, v187, v179, s81\n"
    "v_accvgpr_write_b32 a240, v20\n"
    "v_accvgpr_write_b32 a241, v21\n"
    "v_accvgpr_write_b32 a242, v22\n"
    "v_accvgpr_write_b32 a243, v23\n"
    "s_waitcnt vmcnt(7)\n"
    "s_add_u32 s88, s66, 25600\n"
    "s_addc_u32 s89, s67, 0\n"
    "global_load_dwordx4 v[248:251], v1, s[88:89] nt\n"
    "v_perm_b32 v20, v139, v131, s82\n"
    "v_perm_b32 v21, v155, v147, s82\n"
    "v_perm_b32 v22, v171, v163, s82\n"
    "v_perm_b32 v23, v187, v179, s82\n"
    "v_accvgpr_write_b32 a248, v20\n"
    "v_accvgpr_write_b32 a249, v21\n"
    "v_accvgpr_write_b32 a250, v22\n"
    "v_accvgpr_write_b32 a251, v23\n"
    "s_waitcnt lgkmcnt(0)\n"
    "v_mfma_f32_16x16x32_bf16 v[32:35], v[28:31], a[192:195], v[32:35]\n"
    "v_mfma_f32_16x16x32_bf16 v[36:39], v[28:31], a[200:203], v[36:39]\n"
    "v_mfma_f32_16x16x32_bf16 v[40:43], v[28:31], a[208:211], v[40:43]\n"
    "v_mfma_f32_16x16x32_bf16 v[44:47], v[28:31], a[216:219], v[44:47]\n"
    "v_mfma_f32_16x16x32_bf16 v[48:51], v[28:31], a[224:227], v[48:51]\n"
    "v_mfma_f32_16x16x32_bf16 v[52:55], v[28:31], a[232:235], v[52:55]\n"
    "v_mfma_f32_16x16x32_bf16 v[56:59], v[28:31], a[240:243], v[56:59]\n"
    "v_mfma_f32_16x16x32_bf16 v[60:63], v[28:31], a[248:251], v[60:63]\n"
    "ds_read_b32 v28, v27 offset:16520\n"
    "ds_read_b32 v29, v27 offset:16536\n"
    "ds_read_b32 v30, v27 offset:16552\n"
    "ds_read_b32 v31, v27 offset:16568\n"
    "s_waitcnt vmcnt(8)\n"
    "v_perm_b32 v20, v140, v132, s81\n"
    "v_perm_b32 v21, v156, v148, s81\n"
    "v_perm_b32 v22, v172, v164, s81\n"
    "v_perm_b32 v23, v188, v180, s81\n"
    "v_accvgpr_write_b32 a196, v20\n"
    "v_accvgpr_write_b32 a197, v21\n"
    "v_accvgpr_write_b32 a198, v22\n"
    "v_accvgpr_write_b32 a199, v23\n"
    "v_perm_b32 v20, v140, v132, s82\n"
    "v_perm_b32 v21, v156, v148, s82\n"
    "v_perm_b32 v22, v172, v164, s82\n"
    "v_perm_b32 v23, v188, v180, s82\n"
    "v_accvgpr_write_b32 a204, v20\n"
    "v_accvgpr_write_b32 a205, v21\n"
    "v_accvgpr_write_b32 a206, v22\n"
    "v_accvgpr_write_b32 a207, v23\n"
    "v_perm_b32 v20, v141, v133, s81\n"
    "v_perm_b32 v21, v157, v149, s81\n"
    "v_perm_b32 v22, v173, v165, s81\n"
    "v_perm_b32 v23, v189, v181, s81\n"
    "v_accvgpr_write_b32 a212, v20\n"
    "v_accvgpr_write_b32 a213, v21\n"
    "v_accvgpr_write_b32 a214, v22\n"
    "v_accvgpr_write_b32 a215, v23\n"
    "v_perm_b32 v20, v141, v133, s82\n"
    "v_perm_b32 v21, v157, v149, s82\n"
    "v_perm_b32 v22, v173, v165, s82\n"
    "v_perm_b32 v23, v189, v181, s82\n"
    "v_accvgpr_write_b32 a220, v20\n"
    "v_accvgpr_write_b32 a221, v21\n"
    "v_accvgpr_write_b32 a222, v22\n"
    "v_accvgpr_write_b32 a223, v23\n"
    "v_perm_b32 v20, v142, v134, s81\n"
    "v_perm_b32 v21, v158, v150, s81\n"
    "v_perm_b32 v22, v174, v166, s81\n"
    "v_perm_b32 v23, v190, v182, s81\n"
    "v_accvgpr_write_b32 a228, v20\n"
    "v_accvgpr_write_b32 a229, v21\n"
    "v_accvgpr_write_b32 a230, v22\n"
    "v_accvgpr_write_b32 a231, v23\n"
    "v_perm_b32 v20, v142, v134, s82\n"
    "v_perm_b32 v21, v158, v150, s82\n"
    "v_perm_b32 v22, v174, v166, s82\n"
    "v_perm_b32 v23, v190, v182, s82\n"
    "v_accvgpr_write_b32 a236, v20\n"
    "v_accvgpr_write_b32 a237, v21\n"
    "v_accvgpr_write_b32 a238, v22\n"
    "v_accvgpr_write_b32 a239, v23\n"
    "v_perm_b32 v20, v143, v135, s81\n"
    "v_perm_b32 v21, v159, v151, s81\n"
    "v_perm_b32 v22, v175, v167, s81\n"
    "v_perm_b32 v23, v191, v183, s81\n"
    "v_accvgpr_write_b32 a244, v20\n"
    "v_accvgpr_write_b32 a245, v21\n"
    "v_accvgpr_write_b32 a246, v22\n"
    "v_accvgpr_write_b32 a247, v23\n"
    "v_perm_b32 v20, v143, v135, s82\n"
    "v_perm_b32 v21, v159, v151, s82\n"
    "v_perm_b32 v22, v175, v167, s82\n"
    "v_perm_b32 v23, v191, v183, s82\n"
    "v_accvgpr_write_b32 a252, v20\n"
    "v_accvgpr_write_b32 a253, v21\n"
    "v_accvgpr_write_b32 a254, v22\n"
    "v_accvgpr_write_b32 a255, v23\n"
    "s_waitcnt lgkmcnt(0)\n"
    "v_mfma_f32_16x16x32_bf16 v[32:35], v[28:31], a[196:199], v[32:35]\n"
    "v_mfma_f32_16x16x32_bf16 v[36:39], v[28:31], a[204:207], v[36:39]\n"
    "v_mfma_f32_16x16x32_bf16 v[40:43], v[28:31], a[212:215], v[40:43]\n"
    "v_mfma_f32_16x16x32_bf16 v[44:47], v[28:31], a[220:223], v[44:47]\n"
    "v_mfma_f32_16x16x32_bf16 v[48:51], v[28:31], a[228:231], v[48:51]\n"
    "v_mfma_f32_16x16x32_bf16 v[52:55], v[28:31], a[236:239], v[52:55]\n"
    "v_mfma_f32_16x16x32_bf16 v[56:59], v[28:31], a[244:247], v[56:59]\n"
    "v_mfma_f32_16x16x32_bf16 v[60:63], v[28:31], a[252:255], v[60:63]\n"
    "s_waitcnt vmcnt(7)\n"
    "s_mov_b64 s[66:67], %2\n"
    "s_add_u32 s88, s66, 4096\n"
    "s_addc_u32 s89, s67, 0\n"
    "global_load_dwordx4 v[196:199], v1, s[88:89] nt\n"
    "s_waitcnt vmcnt(7)\n"
    "s_add_u32 s88, s66, 5120\n"
    "s_addc_u32 s89, s67, 0\n"
    "global_load_dwordx4 v[204:207], v1, s[88:89] nt\n"
    "s_waitcnt vmcnt(7)\n"
    "s_add_u32 s88, s66, 12288\n"
    "s_addc_u32 s89, s67, 0\n"
    "global_load_dwordx4 v[212:215], v1, s[88:89] nt\n"
    "v_lshlrev_b32 v27, 1, v0\n"
    "v_lshl_add_u32 v27, v25, 11, v27\n"
    "s_lshl_b32 s57, s51, 12\n"
    "v_add_u32 v27, s57, v27\n"
    "s_mov_b32 exec_hi, 0\n"
    "v_mov_b32 v28, v32\n"
    "v_mov_b32 v29, v36\n"
    "v_mov_b32 v30, v40\n"
    "v_mov_b32 v31, v44\n"
    "ds_write_b128 v27, v[28:31] offset:24576\n"
    "v_mov_b32 v20, v48\n"
    "v_mov_b32 v21, v52\n"
    "v_mov_b32 v22, v56\n"
    "v_mov_b32 v23, v60\n"
    "ds_write_b128 v27, v[20:23] offset:24592\n"
    "v_mov_b32 v28, v33\n"
    "v_mov_b32 v29, v37\n"
    "v_mov_b32 v30, v41\n"
    "v_mov_b32 v31, v45\n"
    "ds_write_b128 v27, v[28:31] offset:25088\n"
    "v_mov_b32 v20, v49\n"
    "v_mov_b32 v21, v53\n"
    "v_mov_b32 v22, v57\n"
    "v_mov_b32 v23, v61\n"
    "ds_write_b128 v27, v[20:23] offset:25104\n"
    "v_mov_b32 v28, v34\n"
    "v_mov_b32 v29, v38\n"
    "v_mov_b32 v30, v42\n"
    "v_mov_b32 v31, v46\n"
    "ds_write_b128 v27, v[28:31] offset:25600\n"
    "v_mov_b32 v20, v50\n"
    "v_mov_b32 v21, v54\n"
    "v_mov_b32 v22, v58\n"
    "v_mov_b32 v23, v62\n"
    "ds_write_b128 v27, v[20:23] offset:25616\n"
    "v_mov_b32 v28, v35\n"
    "v_mov_b32 v29, v39\n"
    "v_mov_b32 v30, v43\n"
    "v_mov_b32 v31, v47\n"
    "ds_write_b128 v27, v[28:31] offset:26112\n"
    "v_mov_b32 v20, v51\n"
    "v_mov_b32 v21, v55\n"
    "v_mov_b32 v22, v59\n"
    "v_mov_b32 v23, v63\n"
    "ds_write_b128 v27, v[20:23] offset:26128\n"
    "v_lshlrev_b32 v27, 1, v0\n"
    "v_lshl_add_u32 v27, v25, 9, v27\n"
    "s_lshl_b32 s57, s51, 10\n"
    "v_add_u32 v27, s57, v27\n"
    "s_waitcnt lgkmcnt(0)\n"
    "s_barrier\n"
    "ds_read_b128 v[32:35], v27 offset:24576\n"
    "ds_read_b128 v[36:39], v27 offset:24592\n"
    "ds_read_b128 v[40:43], v27 offset:28672\n"
    "ds_read_b128 v[44:47], v27 offset:28688\n"
    "ds_read_b128 v[48:51], v27 offset:32768\n"
    "ds_read_b128 v[52:55], v27 offset:32784\n"
    "ds_read_b128 v[56:59], v27 offset:36864\n"
    "ds_read_b128 v[60:63], v27 offset:36880\n"
    "v_lshl_add_u32 v27, v25, 13, v0\n"
    "s_lshl_b32 s56, s51, 14\n"
    "s_lshl_b32 s57, s50, 8\n"
    "s_add_u32 s56, s56, s57\n"
    "s_waitcnt lgkmcnt(0)\n"
    "v_add_f32 v32, v32, v40\n"
    "v_add_f32 v48, v48, v56\n"
    "v_add_f32 v32, v32, v48\n"
    "v_add_f32 v33, v33, v41\n"
    "v_add_f32 v49, v49, v57\n"
    "v_add_f32 v33, v33, v49\n"
    "v_add_f32 v34, v34, v42\n"
    "v_add_f32 v50, v50, v58\n"
    "v_add_f32 v34, v34, v50\n"
    "v_add_f32 v35, v35, v43\n"
    "v_add_f32 v51, v51, v59\n"
    "v_add_f32 v35, v35, v51\n"
    "v_add_f32 v36, v36, v44\n"
    "v_add_f32 v52, v52, v60\n"
    "v_add_f32 v36, v36, v52\n"
    "v_add_f32 v37, v37, v45\n"
    "v_add_f32 v53, v53, v61\n"
    "v_add_f32 v37, v37, v53\n"
    "v_add_f32 v38, v38, v46\n"
    "v_add_f32 v54, v54, v62\n"
    "v_add_f32 v38, v38, v54\n"
    "v_add_f32 v39, v39, v47\n"
    "v_add_f32 v55, v55, v63\n"
    "v_add_f32 v39, v39, v55\n"
    "v_cvt_pk_bf16_f32 v28, v32, v33\n"
    "v_min_u32 v28, -2, v28\n"
    "v_cvt_pk_bf16_f32 v29, v34, v35\n"
    "v_min_u32 v29, -2, v29\n"
    "v_cvt_pk_bf16_f32 v30, v36, v37\n"
    "v_min_u32 v30, -2, v30\n"
    "v_cvt_pk_bf16_f32 v31, v38, v39\n"
    "v_min_u32 v31, -2, v31\n"
    "buffer_store_dwordx4 v[28:31], v27, s[40:43], s56 offen sc1\n"
    "s_mov_b64 exec, -1\n"
    "s_waitcnt vmcnt(7)\n"
    "s_add_u32 s88, s66, 13312\n"
    "s_addc_u32 s89, s67, 0\n"
    "global_load_dwordx4 v[220:223], v1, s[88:89] nt\n"
    "s_waitcnt vmcnt(7)\n"
    "s_add_u32 s88, s66, 20480\n"
    "s_addc_u32 s89, s67, 0\n"
    "global_load_dwordx4 v[228:231], v1, s[88:89] nt\n"
    "s_waitcnt vmcnt(7)\n"
    "s_add_u32 s88, s66, 21504\n"
    "s_addc_u32 s89, s67, 0\n"
    "global_load_dwordx4 v[236:239], v1, s[88:89] nt\n"
    "s_waitcnt vmcnt(7)\n"
    "s_add_u32 s88, s66, 28672\n"
    "s_addc_u32 s89, s67, 0\n"
    "global_load_dwordx4 v[244:247], v1, s[88:89] nt\n"
    "s_waitcnt vmcnt(7)\n"
    "s_add_u32 s88, s66, 29696\n"
    "s_addc_u32 s89, s67, 0\n"
    "global_load_dwordx4 v[252:255], v1, s[88:89] nt\n"
    "v_lshlrev_b32 v27, 5, v14\n"
    "v_and_b32 v27, 0xf80, v27\n"
    "s_mov_b32 s56, 966656\n"
    "buffer_load_dwordx2 v[32:33], v27, s[40:43], s56 offen sc1\n"
    "buffer_load_dword v18, v7, s[44:47], 0 offen\n"
    "s_waitcnt vmcnt(11)\n"
    "s_waitcnt vmcnt(7)\n"
    "s_mov_b64 s[66:67], %2\n"
    "s_add_u32 s88, s66, 65536\n"
    "s_addc_u32 s89, s67, 0\n"
    "global_load_dwordx4 v[64:67], v1, s[88:89] nt\n"
    "v_perm_b32 v20, v200, v192, s81\n"
    "v_perm_b32 v21, v216, v208, s81\n"
    "v_perm_b32 v22, v232, v224, s81\n"
    "v_perm_b32 v23, v248, v240, s81\n"
    "v_accvgpr_write_b32 a0, v20\n"
    "v_accvgpr_write_b32 a1, v21\n"
    "v_accvgpr_write_b32 a2, v22\n"
    "v_accvgpr_write_b32 a3, v23\n"
    "s_waitcnt vmcnt(7)\n"
    "s_add_u32 s88, s66, 66560\n"
    "s_addc_u32 s89, s67, 0\n"
    "global_load_dwordx4 v[72:75], v1, s[88:89] nt\n"
    "v_perm_b32 v20, v200, v192, s82\n"
    "v_perm_b32 v21, v216, v208, s82\n"
    "v_perm_b32 v22, v232, v224, s82\n"
    "v_perm_b32 v23, v248, v240, s82\n"
    "v_accvgpr_write_b32 a8, v20\n"
    "v_accvgpr_write_b32 a9, v21\n"
    "v_accvgpr_write_b32 a10, v22\n"
    "v_accvgpr_write_b32 a11, v23\n"
    "s_waitcnt vmcnt(7)\n"
    "s_add_u32 s88, s66, 73728\n"
    "s_addc_u32 s89, s67, 0\n"
    "global_load_dwordx4 v[80:83], v1, s[88:89] nt\n"
    "v_perm_b32 v20, v201, v193, s81\n"
    "v_perm_b32 v21, v217, v209, s81\n"
    "v_perm_b32 v22, v233, v225, s81\n"
    "v_perm_b32 v23, v249, v241, s81\n"
    "v_accvgpr_write_b32 a16, v20\n"
    "v_accvgpr_write_b32 a17, v21\n"
    "v_accvgpr_write_b32 a18, v22\n"
    "v_accvgpr_write_b32 a19, v23\n"
    "s_waitcnt vmcnt(7)\n"
    "s_add_u32 s88, s66, 74752\n"
    "s_addc_u32 s89, s67, 0\n"
    "global_load_dwordx4 v[88:91], v1, s[88:89] nt\n"
    "v_perm_b32 v20, v201, v193, s82\n"
    "v_perm_b32 v21, v217, v209, s82\n"
    "v_perm_b32 v22, v233, v225, s82\n"
    "v_perm_b32 v23, v249, v241, s82\n"
    "v_accvgpr_write_b32 a24, v20\n"
    "v_accvgpr_write_b32 a25, v21\n"
    "v_accvgpr_write_b32 a26, v22\n"
    "v_accvgpr_write_b32 a27, v23\n"
    "s_waitcnt vmcnt(7)\n"
    "s_add_u32 s88, s66, 81920\n"
    "s_addc_u32 s89, s67, 0\n"
    "global_load_dwordx4 v[96:99], v1, s[88:89] nt\n"
    "v_perm_b32 v20, v202, v194, s81\n"
    "v_perm_b32 v21, v218, v210, s81\n"
    "v_perm_b32 v22, v234, v226, s81\n"
    "v_perm_b32 v23, v250, v242, s81\n"
    "v_accvgpr_write_b32 a32, v20\n"
    "v_accvgpr_write_b32 a33, v21\n"
    "v_accvgpr_write_b32 a34, v22\n"
    "v_accvgpr_write_b32 a35, v23\n"
    "s_waitcnt vmcnt(7)\n"
    "s_add_u32 s88, s66, 82944\n"
    "s_addc_u32 s89, s67, 0\n"
    "global_load_dwordx4 v[104:107], v1, s[88:89] nt\n"
    "v_perm_b32 v20, v202, v194, s82\n"
    "v_perm_b32 v21, v218, v210, s82\n"
    "v_perm_b32 v22, v234, v226, s82\n"
    "v_perm_b32 v23, v250, v242, s82\n"
    "v_accvgpr_write_b32 a40, v20\n"
    "v_accvgpr_write_b32 a41, v21\n"
    "v_accvgpr_write_b32 a42, v22\n"
    "v_accvgpr_write_b32 a43, v23\n"
    "s_waitcnt vmcnt(7)\n"
    "s_add_u32 s88, s66, 90112\n"
    "s_addc_u32 s89, s67, 0\n"
    "global_load_dwordx4 v[112:115], v1, s[88:89] nt\n"
    "v_perm_b32 v20, v203, v195, s81\n"
    "v_perm_b32 v21, v219, v211, s81\n"
    "v_perm_b32 v22, v235, v227, s81\n"
    "v_perm_b32 v23, v251, v243, s81\n"
    "v_accvgpr_write_b32 a48, v20\n"
    "v_accvgpr_write_b32 a49, v21\n"
    "v_accvgpr_write_b32 a50, v22\n"
    "v_accvgpr_write_b32 a51, v23\n"
    "s_waitcnt vmcnt(7)\n"
    "s_add_u32 s88, s66, 91136\n"
    "s_addc_u32 s89, s67, 0\n"
    "global_load_dwordx4 v[120:123], v1, s[88:89] nt\n"
    "v_perm_b32 v20, v203, v195, s82\n"
    "v_perm_b32 v21, v219, v211, s82\n"
    "v_perm_b32 v22, v235, v227, s82\n"
    "v_perm_b32 v23, v251, v243, s82\n"
    "v_accvgpr_write_b32 a56, v20\n"
    "v_accvgpr_write_b32 a57, v21\n"
    "v_accvgpr_write_b32 a58, v22\n"
    "v_accvgpr_write_b32 a59, v23\n"
    "s_waitcnt vmcnt(9)\n"
    "s_nop 0\n"
    "Cfl_%=:\n"
    "v_cmp_ne_u64 vcc, s[92:93], v[32:33]\n"
    "s_nop 4\n"
    "s_cbranch_vccz Cfld_%=\n"
    "s_sub_u32 s55, s55, 1\n"
    "s_cmp_lt_i32 s55, 0\n"
    "s_cbranch_scc1 Cfld_%=\n"
    "buffer_load_dwordx2 v[32:33], v27, s[40:43], s56 offen sc1\n"
    "s_waitcnt vmcnt(0)\n"
    "s_branch Cfl_%=\n"
    "Cfld_%=:\n"
    "s_waitcnt vmcnt(10)\n"
    "s_waitcnt vmcnt(7)\n"
    "s_mov_b64 s[66:67], %2\n"
    "s_add_u32 s88, s66, 69632\n"
    "s_addc_u32 s89, s67, 0\n"
    "global_load_dwordx4 v[68:71], v1, s[88:89] nt\n"
    "v_perm_b32 v20, v204, v196, s81\n"
    "v_perm_b32 v21, v220, v212, s81\n"
    "v_perm_b32 v22, v236, v228, s81\n"
    "v_perm_b32 v23, v252, v244, s81\n"
    "v_accvgpr_write_b32 a4, v20\n"
    "v_accvgpr_write_b32 a5, v21\n"
    "v_accvgpr_write_b32 a6, v22\n"
    "v_accvgpr_write_b32 a7, v23\n"
    "s_waitcnt vmcnt(7)\n"
    "s_add_u32 s88, s66, 70656\n"
    "s_addc_u32 s89, s67, 0\n"
    "global_load_dwordx4 v[76:79], v1, s[88:89] nt\n"
    "v_perm_b32 v20, v204, v196, s82\n"
    "v_perm_b32 v21, v220, v212, s82\n"
    "v_perm_b32 v22, v236, v228, s82\n"
    "v_perm_b32 v23, v252, v244, s82\n"
    "v_accvgpr_write_b32 a12, v20\n"
    "v_accvgpr_write_b32 a13, v21\n"
    "v_accvgpr_write_b32 a14, v22\n"
    "v_accvgpr_write_b32 a15, v23\n"
    "s_waitcnt vmcnt(7)\n"
    "s_add_u32 s88, s66, 77824\n"
    "s_addc_u32 s89, s67, 0\n"
    "global_load_dwordx4 v[84:87], v1, s[88:89] nt\n"
    "v_perm_b32 v20, v205, v197, s81\n"
    "v_perm_b32 v21, v221, v213, s81\n"
    "v_perm_b32 v22, v237, v229, s81\n"
    "v_perm_b32 v23, v253, v245, s81\n"
    "v_accvgpr_write_b32 a20, v20\n"
    "v_accvgpr_write_b32 a21, v21\n"
    "v_accvgpr_write_b32 a22, v22\n"
    "v_accvgpr_write_b32 a23, v23\n"
    "s_waitcnt vmcnt(7)\n"
    "s_add_u32 s88, s66, 78848\n"
    "s_addc_u32 s89, s67, 0\n"
    "global_load_dwordx4 v[92:95], v1, s[88:89] nt\n"
    "v_perm_b32 v20, v205, v197, s82\n"
    "v_perm_b32 v21, v221, v213, s82\n"
    "v_perm_b32 v22, v237, v229, s82\n"
    "v_perm_b32 v23, v253, v245, s82\n"
    "v_accvgpr_write_b32 a28, v20\n"
    "v_accvgpr_write_b32 a29, v21\n"
    "v_accvgpr_write_b32 a30, v22\n"
    "v_accvgpr_write_b32 a31, v23\n"
    "s_waitcnt vmcnt(7)\n"
    "s_add_u32 s88, s66, 86016\n"
    "s_addc_u32 s89, s67, 0\n"
    "global_load_dwordx4 v[100:103], v1, s[88:89] nt\n"
    "v_perm_b32 v20, v206, v198, s81\n"
    "v_perm_b32 v21, v222, v214, s81\n"
    "v_perm_b32 v22, v238, v230, s81\n"
    "v_perm_b32 v23, v254, v246, s81\n"
    "v_accvgpr_write_b32 a36, v20\n"
    "v_accvgpr_write_b32 a37, v21\n"
    "v_accvgpr_write_b32 a38, v22\n"
    "v_accvgpr_write_b32 a39, v23\n"
    "s_waitcnt vmcnt(7)\n"
    "s_add_u32 s88, s66, 87040\n"
    "s_addc_u32 s89, s67, 0\n"
    "global_load_dwordx4 v[108:111], v1, s[88:89] nt\n"
    "v_perm_b32 v20, v206, v198, s82\n"
    "v_perm_b32 v21, v222, v214, s82\n"
    "v_perm_b32 v22, v238, v230, s82\n"
    "v_perm_b32 v23, v254, v246, s82\n"
    "v_accvgpr_write_b32 a44, v20\n"
    "v_accvgpr_write_b32 a45, v21\n"
    "v_accvgpr_write_b32 a46, v22\n"
    "v_accvgpr_write_b32 a47, v23\n"
    "s_waitcnt vmcnt(7)\n"
    "s_add_u32 s88, s66, 94208\n"
    "s_addc_u32 s89, s67, 0\n"
    "global_load_dwordx4 v[116:119], v1, s[88:89] nt\n"
    "v_perm_b32 v20, v207, v199, s81\n"
    "v_perm_b32 v21, v223, v215, s81\n"
    "v_perm_b32 v22, v239, v231, s81\n"
    "v_perm_b32 v23, v255, v247, s81\n"
    "v_accvgpr_write_b32 a52, v20\n"
    "v_accvgpr_write_b32 a53, v21\n"
    "v_accvgpr_write_b32 a54, v22\n"
    "v_accvgpr_write_b32 a55, v23\n"
    "s_waitcnt vmcnt(7)\n"
    "s_add_u32 s88, s66, 95232\n"
    "s_addc_u32 s89, s67, 0\n"
    "global_load_dwordx4 v[124:127], v1, s[88:89] nt\n"
    "v_perm_b32 v20, v207, v199, s82\n"
    "v_perm_b32 v21, v223, v215, s82\n"
    "v_perm_b32 v22, v239, v231, s82\n"
    "v_perm_b32 v23, v255, v247, s82\n"
    "v_accvgpr_write_b32 a60, v20\n"
    "v_accvgpr_write_b32 a61, v21\n"
    "v_accvgpr_write_b32 a62, v22\n"
    "v_accvgpr_write_b32 a63, v23\n"
    "s_cmp_ge_u32 s95, 22\n"
    "s_nop 0\n"
    "s_cbranch_scc1 Cpnf_%=\n"
    "s_waitcnt vmcnt(8)\n"
    "s_waitcnt vmcnt(7)\n"
    "s_mov_b32 s88, 0\n"
    "s_mov_b32 s89, 0\n"
    "v_lshl_add_u64 v[4:5], v[2:3], 0, s[88:89]\n"
    "global_load_dwordx4 v[128:131], v[4:5], off nt\n"
    "v_perm_b32 v20, v72, v64, s81\n"
    "v_perm_b32 v21, v88, v80, s81\n"
    "v_perm_b32 v22, v104, v96, s81\n"
    "v_perm_b32 v23, v120, v112, s81\n"
    "v_accvgpr_write_b32 a64, v20\n"
    "v_accvgpr_write_b32 a65, v21\n"
    "v_accvgpr_write_b32 a66, v22\n"
    "v_accvgpr_write_b32 a67, v23\n"
    "s_waitcnt vmcnt(7)\n"
    "global_load_dwordx4 v[136:139], v[4:5], off offset:2752 nt\n"
    "v_perm_b32 v20, v72, v64, s82\n"
    "v_perm_b32 v21, v88, v80, s82\n"
    "v_perm_b32 v22, v104, v96, s82\n"
    "v_perm_b32 v23, v120, v112, s82\n"
    "v_accvgpr_write_b32 a72, v20\n"
    "v_accvgpr_write_b32 a73, v21\n"
    "v_accvgpr_write_b32 a74, v22\n"
    "v_accvgpr_write_b32 a75, v23\n"
    "s_waitcnt vmcnt(7)\n"
    "s_mov_b32 s88, 22016\n"
    "s_mov_b32 s89, 0\n"
    "v_lshl_add_u64 v[4:5], v[2:3], 0, s[88:89]\n"
    "global_load_dwordx4 v[144:147], v[4:5], off nt\n"
    "v_perm_b32 v20, v73, v65, s81\n"
    "v_perm_b32 v21, v89, v81, s81\n"
    "v_perm_b32 v22, v105, v97, s81\n"
    "v_perm_b32 v23, v121, v113, s81\n"
    "v_accvgpr_write_b32 a80, v20\n"
    "v_accvgpr_write_b32 a81, v21\n"
    "v_accvgpr_write_b32 a82, v22\n"
    "v_accvgpr_write_b32 a83, v23\n"
    "s_waitcnt vmcnt(7)\n"
    "global_load_dwordx4 v[152:155], v[4:5], off offset:2752 nt\n"
    "v_perm_b32 v20, v73, v65, s82\n"
    "v_perm_b32 v21, v89, v81, s82\n"
    "v_perm_b32 v22, v105, v97, s82\n"
    "v_perm_b32 v23, v121, v113, s82\n"
    "v_accvgpr_write_b32 a88, v20\n"
    "v_accvgpr_write_b32 a89, v21\n"
    "v_accvgpr_write_b32 a90, v22\n"
    "v_accvgpr_write_b32 a91, v23\n"
    "s_waitcnt vmcnt(7)\n"
    "s_mov_b32 s88, 44032\n"
    "s_mov_b32 s89, 0\n"
    "v_lshl_add_u64 v[4:5], v[2:3], 0, s[88:89]\n"
    "global_load_dwordx4 v[160:163], v[4:5], off nt\n"
    "v_perm_b32 v20, v74, v66, s81\n"
    "v_perm_b32 v21, v90, v82, s81\n"
    "v_perm_b32 v22, v106, v98, s81\n"
    "v_perm_b32 v23, v122, v114, s81\n"
    "v_accvgpr_write_b32 a96, v20\n"
    "v_accvgpr_write_b32 a97, v21\n"
    "v_accvgpr_write_b32 a98, v22\n"
    "v_accvgpr_write_b32 a99, v23\n"
    "s_waitcnt vmcnt(7)\n"
    "global_load_dwordx4 v[168:171], v[4:5], off offset:2752 nt\n"
    "v_perm_b32 v20, v74, v66, s82\n"
    "v_perm_b32 v21, v90, v82, s82\n"
    "v_perm_b32 v22, v106, v98, s82\n"
    "v_perm_b32 v23, v122, v114, s82\n"
    "v_accvgpr_write_b32 a104, v20\n"
    "v_accvgpr_write_b32 a105, v21\n"
    "v_accvgpr_write_b32 a106, v22\n"
    "v_accvgpr_write_b32 a107, v23\n"
    "s_waitcnt vmcnt(7)\n"
    "s_mov_b32 s88, 66048\n"
    "s_mov_b32 s89, 0\n"
    "v_lshl_add_u64 v[4:5], v[2:3], 0, s[88:89]\n"
    "global_load_dwordx4 v[176:179], v[4:5], off nt\n"
    "v_perm_b32 v20, v75, v67, s81\n"
    "v_perm_b32 v21, v91, v83, s81\n"
    "v_perm_b32 v22, v107, v99, s81\n"
    "v_perm_b32 v23, v123, v115, s81\n"
    "v_accvgpr_write_b32 a112, v20\n"
    "v_accvgpr_write_b32 a113, v21\n"
    "v_accvgpr_write_b32 a114, v22\n"
    "v_accvgpr_write_b32 a115, v23\n"
    "s_waitcnt vmcnt(7)\n"
    "global_load_dwordx4 v[184:187], v[4:5], off offset:2752 nt\n"
    "v_perm_b32 v20, v75, v67, s82\n"
    "v_perm_b32 v21, v91, v83, s82\n"
    "v_perm_b32 v22, v107, v99, s82\n"
    "v_perm_b32 v23, v123, v115, s82\n"
    "v_accvgpr_write_b32 a120, v20\n"
    "v_accvgpr_write_b32 a121, v21\n"
    "v_accvgpr_write_b32 a122, v22\n"
    "v_accvgpr_write_b32 a123, v23\n"
    "s_waitcnt vmcnt(8)\n"
    "s_waitcnt vmcnt(7)\n"
    "s_mov_b32 s88, 11008\n"
    "s_mov_b32 s89, 0\n"
    "v_lshl_add_u64 v[4:5], v[2:3], 0, s[88:89]\n"
    "global_load_dwordx4 v[132:135], v[4:5], off nt\n"
    "v_perm_b32 v20, v76, v68, s81\n"
    "v_perm_b32 v21, v92, v84, s81\n"
    "v_perm_b32 v22, v108, v100, s81\n"
    "v_perm_b32 v23, v124, v116, s81\n"
    "v_accvgpr_write_b32 a68, v20\n"
    "v_accvgpr_write_b32 a69, v21\n"
    "v_accvgpr_write_b32 a70, v22\n"
    "v_accvgpr_write_b32 a71, v23\n"
    "s_waitcnt vmcnt(7)\n"
    "global_load_dwordx4 v[140:143], v[4:5], off offset:2752 nt\n"
    "v_perm_b32 v20, v76, v68, s82\n"
    "v_perm_b32 v21, v92, v84, s82\n"
    "v_perm_b32 v22, v108, v100, s82\n"
    "v_perm_b32 v23, v124, v116, s82\n"
    "v_accvgpr_write_b32 a76, v20\n"
    "v_accvgpr_write_b32 a77, v21\n"
    "v_accvgpr_write_b32 a78, v22\n"
    "v_accvgpr_write_b32 a79, v23\n"
    "s_waitcnt vmcnt(7)\n"
    "s_mov_b32 s88, 33024\n"
    "s_mov_b32 s89, 0\n"
    "v_lshl_add_u64 v[4:5], v[2:3], 0, s[88:89]\n"
    "global_load_dwordx4 v[148:151], v[4:5], off nt\n"
    "v_perm_b32 v20, v77, v69, s81\n"
    "v_perm_b32 v21, v93, v85, s81\n"
    "v_perm_b32 v22, v109, v101, s81\n"
    "v_perm_b32 v23, v125, v117, s81\n"
    "v_accvgpr_write_b32 a84, v20\n"
    "v_accvgpr_write_b32 a85, v21\n"
    "v_accvgpr_write_b32 a86, v22\n"
    "v_accvgpr_write_b32 a87, v23\n"
    "s_waitcnt vmcnt(7)\n"
    "global_load_dwordx4 v[156:159], v[4:5], off offset:2752 nt\n"
    "v_perm_b32 v20, v77, v69, s82\n"
    "v_perm_b32 v21, v93, v85, s82\n"
    "v_perm_b32 v22, v109, v101, s82\n"
    "v_perm_b32 v23, v125, v117, s82\n"
    "v_accvgpr_write_b32 a92, v20\n"
    "v_accvgpr_write_b32 a93, v21\n"
    "v_accvgpr_write_b32 a94, v22\n"
    "v_accvgpr_write_b32 a95, v23\n"
    "s_waitcnt vmcnt(7)\n"
    "s_mov_b32 s88, 55040\n"
    "s_mov_b32 s89, 0\n"
    "v_lshl_add_u64 v[4:5], v[2:3], 0, s[88:89]\n"
    "global_load_dwordx4 v[164:167], v[4:5], off nt\n"
    "v_perm_b32 v20, v78, v70, s81\n"
    "v_perm_b32 v21, v94, v86, s81\n"
    "v_perm_b32 v22, v110, v102, s81\n"
    "v_perm_b32 v23, v126, v118, s81\n"
    "v_accvgpr_write_b32 a100, v20\n"
    "v_accvgpr_write_b32 a101, v21\n"
    "v_accvgpr_write_b32 a102, v22\n"
    "v_accvgpr_write_b32 a103, v23\n"
    "s_waitcnt vmcnt(7)\n"
    "global_load_dwordx4 v[172:175], v[4:5], off offset:2752 nt\n"
    "v_perm_b32 v20, v78, v70, s82\n"
    "v_perm_b32 v21, v94, v86, s82\n"
    "v_perm_b32 v22, v110, v102, s82\n"
    "v_perm_b32 v23, v126, v118, s82\n"
    "v_accvgpr_write_b32 a108, v20\n"
    "v_accvgpr_write_b32 a109, v21\n"
    "v_accvgpr_write_b32 a110, v22\n"
    "v_accvgpr_write_b32 a111, v23\n"
    "s_waitcnt vmcnt(7)\n"
    "s_mov_b32 s88, 77056\n"
    "s_mov_b32 s89, 0\n"
    "v_lshl_add_u64 v[4:5], v[2:3], 0, s[88:89]\n"
    "global_load_dwordx4 v[180:183], v[4:5], off nt\n"
    "v_perm_b32 v20, v79, v71, s81\n"
    "v_perm_b32 v21, v95, v87, s81\n"
    "v_perm_b32 v22, v111, v103, s81\n"
    "v_perm_b32 v23, v127, v119, s81\n"
    "v_accvgpr_write_b32 a116, v20\n"
    "v_accvgpr_write_b32 a117, v21\n"
    "v_accvgpr_write_b32 a118, v22\n"
    "v_accvgpr_write_b32 a119, v23\n"
    "s_waitcnt vmcnt(7)\n"
    "global_load_dwordx4 v[188:191], v[4:5], off offset:2752 nt\n"
    "v_perm_b32 v20, v79, v71, s82\n"
    "v_perm_b32 v21, v95, v87, s82\n"
    "v_perm_b32 v22, v111, v103, s82\n"
    "v_perm_b32 v23, v127, v119, s82\n"
    "v_accvgpr_write_b32 a124, v20\n"
    "v_accvgpr_write_b32 a125, v21\n"
    "v_accvgpr_write_b32 a126, v22\n"
    "v_accvgpr_write_b32 a127, v23\n"
    "s_waitcnt vmcnt(8)\n"
    "s_waitcnt vmcnt(7)\n"
    "s_mov_b32 s88, 176128\n"
    "s_mov_b32 s89, 0\n"
    "v_lshl_add_u64 v[4:5], v[2:3], 0, s[88:89]\n"
    "global_load_dwordx4 v[192:195], v[4:5], off nt\n"
    "v_perm_b32 v20, v136, v128, s81\n"
    "v_perm_b32 v136, v136, v128, s82\n"
    "v_mov_b32 v128, v20\n"
    "s_waitcnt vmcnt(7)\n"
    "global_load_dwordx4 v[200:203], v[4:5], off offset:2752 nt\n"
    "v_perm_b32 v20, v137, v129, s81\n"
    "v_perm_b32 v21, v137, v129, s82\n"
    "v_perm_b32 v129, v152, v144, s81\n"
    "v_perm_b32 v137, v152, v144, s82\n"
    "v_mov_b32 v144, v20\n"
    "v_mov_b32 v152, v21\n"
    "s_waitcnt vmcnt(7)\n"
    "s_mov_b32 s88, 198144\n"
    "s_mov_b32 s89, 0\n"
    "v_lshl_add_u64 v[4:5], v[2:3], 0, s[88:89]\n"
    "global_load_dwordx4 v[208:211], v[4:5], off nt\n"
    "v_perm_b32 v20, v138, v130, s81\n"
    "v_perm_b32 v21, v138, v130, s82\n"
    "v_perm_b32 v130, v168, v160, s81\n"
    "v_perm_b32 v138, v168, v160, s82\n"
    "v_mov_b32 v160, v20\n"
    "v_mov_b32 v168, v21\n"
    "s_waitcnt vmcnt(7)\n"
    "global_load_dwordx4 v[216:219], v[4:5], off offset:2752 nt\n"
    "v_perm_b32 v20, v139, v131, s81\n"
    "v_perm_b32 v21, v139, v131, s82\n"
    "v_perm_b32 v131, v184, v176, s81\n"
    "v_perm_b32 v139, v184, v176, s82\n"
    "v_mov_b32 v176, v20\n"
    "v_mov_b32 v184, v21\n"
    "s_waitcnt vmcnt(7)\n"
    "s_mov_b32 s88, 220160\n"
    "s_mov_b32 s89, 0\n"
    "v_lshl_add_u64 v[4:5], v[2:3], 0, s[88:89]\n"
    "global_load_dwordx4 v[224:227], v[4:5], off nt\n"
    "v_perm_b32 v20, v153, v145, s81\n"
    "v_perm_b32 v153, v153, v145, s82\n"
    "v_mov_b32 v145, v20\n"
    "s_waitcnt vmcnt(7)\n"
    "global_load_dwordx4 v[232:235], v[4:5], off offset:2752 nt\n"
    "v_perm_b32 v20, v154, v146, s81\n"
    "v_perm_b32 v21, v154, v146, s82\n"
    "v_perm_b32 v146, v169, v161, s81\n"
    "v_perm_b32 v154, v169, v161, s82\n"
    "v_mov_b32 v161, v20\n"
    "v_mov_b32 v169, v21\n"
    "s_waitcnt vmcnt(7)\n"
    "s_mov_b32 s88, 242176\n"
    "s_mov_b32 s89, 0\n"
    "v_lshl_add_u64 v[4:5], v[2:3], 0, s[88:89]\n"
    "global_load_dwordx4 v[240:243], v[4:5], off nt\n"
    "v_perm_b32 v20, v155, v147, s81\n"
    "v_perm_b32 v21, v155, v147, s82\n"
    "v_perm_b32 v147, v185, v177, s81\n"
    "v_perm_b32 v155, v185, v177, s82\n"
    "v_mov_b32 v177, v20\n"
    "v_mov_b32 v185, v21\n"
    "s_waitcnt vmcnt(7)\n"
    "global_load_dwordx4 v[248:251], v[4:5], off offset:2752 nt\n"
    "v_perm_b32 v20, v170, v162, s81\n"
    "v_perm_b32 v170, v170, v162, s82\n"
    "v_mov_b32 v162, v20\n"
    "v_perm_b32 v20, v171, v163, s81\n"
    "v_perm_b32 v21, v171, v163, s82\n"
    "v_perm_b32 v163, v186, v178, s81\n"
    "v_perm_b32 v171, v186, v178, s82\n"
    "v_mov_b32 v178, v20\n"
    "v_mov_b32 v186, v21\n"
    "v_perm_b32 v20, v187, v179, s81\n"
    "v_perm_b32 v187, v187, v179, s82\n"
    "v_mov_b32 v179, v20\n"
    "s_waitcnt vmcnt(8)\n"
    "s_waitcnt vmcnt(7)\n"
    "s_mov_b32 s88, 187136\n"
    "s_mov_b32 s89, 0\n"
    "v_lshl_add_u64 v[4:5], v[2:3], 0, s[88:89]\n"
    "global_load_dwordx4 v[196:199], v[4:5], off nt\n"
    "v_perm_b32 v20, v140, v132, s81\n"
    "v_perm_b32 v140, v140, v132, s82\n"
    "v_mov_b32 v132, v20\n"
    "s_waitcnt vmcnt(7)\n"
    "global_load_dwordx4 v[204:207], v[4:5], off offset:2752 nt\n"
    "v_perm_b32 v20, v141, v133, s81\n"
    "v_perm_b32 v21, v141, v133, s82\n"
    "v_perm_b32 v133, v156, v148, s81\n"
    "v_perm_b32 v141, v156, v148, s82\n"
    "v_mov_b32 v148, v20\n"
    "v_mov_b32 v156, v21\n"
    "s_waitcnt vmcnt(7)\n"
    "s_mov_b32 s88, 209152\n"
    "s_mov_b32 s89, 0\n"
    "v_lshl_add_u64 v[4:5], v[2:3], 0, s[88:89]\n"
    "global_load_dwordx4 v[212:215], v[4:5], off nt\n"
    "v_perm_b32 v20, v142, v134, s81\n"
    "v_perm_b32 v21, v142, v134, s82\n"
    "v_perm_b32 v134, v172, v164, s81\n"
    "v_perm_b32 v142, v172, v164, s82\n"
    "v_mov_b32 v164, v20\n"
    "v_mov_b32 v172, v21\n"
    "s_waitcnt vmcnt(7)\n"
    "global_load_dwordx4 v[220:223], v[4:5], off offset:2752 nt\n"
    "v_perm_b32 v20, v143, v135, s81\n"
    "v_perm_b32 v21, v143, v135, s82\n"
    "v_perm_b32 v135, v188, v180, s81\n"
    "v_perm_b32 v143, v188, v180, s82\n"
    "v_mov_b32 v180, v20\n"
    "v_mov_b32 v188, v21\n"
    "s_waitcnt vmcnt(7)\n"
    "s_mov_b32 s88, 231168\n"
    "s_mov_b32 s89, 0\n"
    "v_lshl_add_u64 v[4:5], v[2:3], 0, s[88:89]\n"
    "global_load_dwordx4 v[228:231], v[4:5], off nt\n"
    "v_perm_b32 v20, v157, v149, s81\n"
    "v_perm_b32 v157, v157, v149, s82\n"
    "v_mov_b32 v149, v20\n"
    "s_waitcnt vmcnt(7)\n"
    "global_load_dwordx4 v[236:239], v[4:5], off offset:2752 nt\n"
    "v_perm_b32 v20, v158, v150, s81\n"
    "v_perm_b32 v21, v158, v150, s82\n"
    "v_perm_b32 v150, v173, v165, s81\n"
    "v_perm_b32 v158, v173, v165, s82\n"
    "v_mov_b32 v165, v20\n"
    "v_mov_b32 v173, v21\n"
    "s_waitcnt vmcnt(7)\n"
    "s_mov_b32 s88, 253184\n"
    "s_mov_b32 s89, 0\n"
    "v_lshl_add_u64 v[4:5], v[2:3], 0, s[88:89]\n"
    "global_load_dwordx4 v[244:247], v[4:5], off nt\n"
    "v_perm_b32 v20, v159, v151, s81\n"
    "v_perm_b32 v21, v159, v151, s82\n"
    "v_perm_b32 v151, v189, v181, s81\n"
    "v_perm_b32 v159, v189, v181, s82\n"
    "v_mov_b32 v181, v20\n"
    "v_mov_b32 v189, v21\n"
    "s_waitcnt vmcnt(7)\n"
    "global_load_dwordx4 v[252:255], v[4:5], off offset:2752 nt\n"
    "v_perm_b32 v20, v174, v166, s81\n"
    "v_perm_b32 v174, v174, v166, s82\n"
    "v_mov_b32 v166, v20\n"
    "v_perm_b32 v20, v175, v167, s81\n"
    "v_perm_b32 v21, v175, v167, s82\n"
    "v_perm_b32 v167, v190, v182, s81\n"
    "v_perm_b32 v175, v190, v182, s82\n"
    "v_mov_b32 v182, v20\n"
    "v_mov_b32 v190, v21\n"
    "v_perm_b32 v20, v191, v183, s81\n"
    "v_perm_b32 v191, v191, v183, s82\n"
    "v_mov_b32 v183, v20\n"
    "s_waitcnt vmcnt(8)\n"
    "s_waitcnt vmcnt(7)\n"
    "s_mov_b64 s[66:67], %5\n"
    "s_add_u32 s88, s66, 0\n"
    "s_addc_u32 s89, s67, 0\n"
    "global_load_dwordx4 v[64:67], v15, s[88:89] nt\n"
    "v_perm_b32 v20, v200, v192, s81\n"
    "v_perm_b32 v200, v200, v192, s82\n"
    "v_mov_b32 v192, v20\n"
    "s_waitcnt vmcnt(7)\n"
    "s_add_u32 s88, s66, 1024\n"
    "s_addc_u32 s89, s67, 0\n"
    "global_load_dwordx4 v[72:75], v15, s[88:89] nt\n"
    "v_perm_b32 v20, v201, v193, s81\n"
    "v_perm_b32 v21, v201, v193, s82\n"
    "v_perm_b32 v193, v216, v208, s81\n"
    "v_perm_b32 v201, v216, v208, s82\n"
    "v_mov_b32 v208, v20\n"
    "v_mov_b32 v216, v21\n"
    "s_waitcnt vmcnt(7)\n"
    "s_add_u32 s88, s66, 2048\n"
    "s_addc_u32 s89, s67, 0\n"
    "global_load_dwordx4 v[80:83], v15, s[88:89] nt\n"
    "v_perm_b32 v20, v202, v194, s81\n"
    "v_perm_b32 v21, v202, v194, s82\n"
    "v_perm_b32 v194, v232, v224, s81\n"
    "v_perm_b32 v202, v232, v224, s82\n"
    "v_mov_b32 v224, v20\n"
    "v_mov_b32 v232, v21\n"
    "s_waitcnt vmcnt(7)\n"
    "s_add_u32 s88, s66, 3072\n"
    "s_addc_u32 s89, s67, 0\n"
    "global_load_dwordx4 v[88:91], v15, s[88:89] nt\n"
    "v_perm_b32 v20, v203, v195, s81\n"
    "v_perm_b32 v21, v203, v195, s82\n"
    "v_perm_b32 v195, v248, v240, s81\n"
    "v_perm_b32 v203, v248, v240, s82\n"
    "v_mov_b32 v240, v20\n"
    "v_mov_b32 v248, v21\n"
    "s_waitcnt vmcnt(7)\n"
    "s_add_u32 s88, s66, 4096\n"
    "s_addc_u32 s89, s67, 0\n"
    "global_load_dwordx4 v[96:99], v15, s[88:89] nt\n"
    "v_perm_b32 v20, v217, v209, s81\n"
    "v_perm_b32 v217, v217, v209, s82\n"
    "v_mov_b32 v209, v20\n"
    "s_waitcnt vmcnt(7)\n"
    "s_add_u32 s88, s66, 5120\n"
    "s_addc_u32 s89, s67, 0\n"
    "global_load_dwordx4 v[104:107], v15, s[88:89] nt\n"
    "v_perm_b32 v20, v218, v210, s81\n"
    "v_perm_b32 v21, v218, v210, s82\n"
    "v_perm_b32 v210, v233, v225, s81\n"
    "v_perm_b32 v218, v233, v225, s82\n"
    "v_mov_b32 v225, v20\n"
    "v_mov_b32 v233, v21\n"
    "s_waitcnt vmcnt(7)\n"
    "s_add_u32 s88, s66, 6144\n"
    "s_addc_u32 s89, s67, 0\n"
    "global_load_dwordx4 v[112:115], v15, s[88:89] nt\n"
    "v_perm_b32 v20, v219, v211, s81\n"
    "v_perm_b32 v21, v219, v211, s82\n"
    "v_perm_b32 v211, v249, v241, s81\n"
    "v_perm_b32 v219, v249, v241, s82\n"
    "v_mov_b32 v241, v20\n"
    "v_mov_b32 v249, v21\n"
    "s_waitcnt vmcnt(7)\n"
    "s_add_u32 s88, s66, 7168\n"
    "s_addc_u32 s89, s67, 0\n"
    "global_load_dwordx4 v[120:123], v15, s[88:89] nt\n"
    "v_perm_b32 v20, v234, v226, s81\n"
    "v_perm_b32 v234, v234, v226, s82\n"
    "v_mov_b32 v226, v20\n"
    "v_perm_b32 v20, v235, v227, s81\n"
    "v_perm_b32 v21, v235, v227, s82\n"
    "v_perm_b32 v227, v250, v242, s81\n"
    "v_perm_b32 v235, v250, v242, s82\n"
    "v_mov_b32 v242, v20\n"
    "v_mov_b32 v250, v21\n"
    "v_perm_b32 v20, v251, v243, s81\n"
    "v_perm_b32 v251, v251, v243, s82\n"
    "v_mov_b32 v243, v20\n"
    "s_waitcnt vmcnt(7)\n"
    "s_mov_b64 s[66:67], %5\n"
    "s_add_u32 s88, s66, 0\n"
    "s_addc_u32 s89, s67, 0\n"
    "global_load_dwordx4 v[68:71], v16, s[88:89] nt\n"
    "s_waitcnt vmcnt(7)\n"
    "s_add_u32 s88, s66, 1024\n"
    "s_addc_u32 s89, s67, 0\n"
    "global_load_dwordx4 v[76:79], v16, s[88:89] nt\n"
    "s_waitcnt vmcnt(7)\n"
    "s_add_u32 s88, s66, 2048\n"
    "s_addc_u32 s89, s67, 0\n"
    "global_load_dwordx4 v[84:87], v16, s[88:89] nt\n"
    "s_waitcnt vmcnt(7)\n"
    "s_add_u32 s88, s66, 3072\n"
    "s_addc_u32 s89, s67, 0\n"
    "global_load_dwordx4 v[92:95], v16, s[88:89] nt\n"
    "s_waitcnt vmcnt(7)\n"
    "s_add_u32 s88, s66, 4096\n"
    "s_addc_u32 s89, s67, 0\n"
    "global_load_dwordx4 v[100:103], v16, s[88:89] nt\n"
    "s_waitcnt vmcnt(7)\n"
    "s_add_u32 s88, s66, 5120\n"
    "s_addc_u32 s89, s67, 0\n"
    "global_load_dwordx4 v[108:111], v16, s[88:89] nt\n"
    "s_waitcnt vmcnt(7)\n"
    "s_add_u32 s88, s66, 6144\n"
    "s_addc_u32 s89, s67, 0\n"
    "global_load_dwordx4 v[116:119], v16, s[88:89] nt\n"
    "s_waitcnt vmcnt(7)\n"
    "s_add_u32 s88, s66, 7168\n"
    "s_addc_u32 s89, s67, 0\n"
    "global_load_dwordx4 v[124:127], v16, s[88:89] nt\n"
    "s_branch Cpdone_%=\n"
    "Cpnf_%=:\n"
    "s_waitcnt vmcnt(8)\n"
    "v_perm_b32 v20, v72, v64, s81\n"
    "v_perm_b32 v21, v88, v80, s81\n"
    "v_perm_b32 v22, v104, v96, s81\n"
    "v_perm_b32 v23, v120, v112, s81\n"
    "v_accvgpr_write_b32 a64, v20\n"
    "v_accvgpr_write_b32 a65, v21\n"
    "v_accvgpr_write_b32 a66, v22\n"
    "v_accvgpr_write_b32 a67, v23\n"
    "v_perm_b32 v20, v72, v64, s82\n"
    "v_perm_b32 v21, v88, v80, s82\n"
    "v_perm_b32 v22, v104, v96, s82\n"
    "v_perm_b32 v23, v120, v112, s82\n"
    "v_accvgpr_write_b32 a72, v20\n"
    "v_accvgpr_write_b32 a73, v21\n"
    "v_accvgpr_write_b32 a74, v22\n"
    "v_accvgpr_write_b32 a75, v23\n"
    "v_perm_b32 v20, v73, v65, s81\n"
    "v_perm_b32 v21, v89, v81, s81\n"
    "v_perm_b32 v22, v105, v97, s81\n"
    "v_perm_b32 v23, v121, v113, s81\n"
    "v_accvgpr_write_b32 a80, v20\n"
    "v_accvgpr_write_b32 a81, v21\n"
    "v_accvgpr_write_b32 a82, v22\n"
    "v_accvgpr_write_b32 a83, v23\n"
    "v_perm_b32 v20, v73, v65, s82\n"
    "v_perm_b32 v21, v89, v81, s82\n"
    "v_perm_b32 v22, v105, v97, s82\n"
    "v_perm_b32 v23, v121, v113, s82\n"
    "v_accvgpr_write_b32 a88, v20\n"
    "v_accvgpr_write_b32 a89, v21\n"
    "v_accvgpr_write_b32 a90, v22\n"
    "v_accvgpr_write_b32 a91, v23\n"
    "v_perm_b32 v20, v74, v66, s81\n"
    "v_perm_b32 v21, v90, v82, s81\n"
    "v_perm_b32 v22, v106, v98, s81\n"
    "v_perm_b32 v23, v122, v114, s81\n"
    "v_accvgpr_write_b32 a96, v20\n"
    "v_accvgpr_write_b32 a97, v21\n"
    "v_accvgpr_write_b32 a98, v22\n"
    "v_accvgpr_write_b32 a99, v23\n"
    "v_perm_b32 v20, v74, v66, s82\n"
    "v_perm_b32 v21, v90, v82, s82\n"
    "v_perm_b32 v22, v106, v98, s82\n"
    "v_perm_b32 v23, v122, v114, s82\n"
    "v_accvgpr_write_b32 a104, v20\n"
    "v_accvgpr_write_b32 a105, v21\n"
    "v_accvgpr_write_b32 a106, v22\n"
    "v_accvgpr_write_b32 a107, v23\n"
    "v_perm_b32 v20, v75, v67, s81\n"
    "v_perm_b32 v21, v91, v83, s81\n"
    "v_perm_b32 v22, v107, v99, s81\n"
    "v_perm_b32 v23, v123, v115, s81\n"
    "v_accvgpr_write_b32 a112, v20\n"
    "v_accvgpr_write_b32 a113, v21\n"
    "v_accvgpr_write_b32 a114, v22\n"
    "v_accvgpr_write_b32 a115, v23\n"
    "v_perm_b32 v20, v75, v67, s82\n"
    "v_perm_b32 v21, v91, v83, s82\n"
    "v_perm_b32 v22, v107, v99, s82\n"
    "v_perm_b32 v23, v123, v115, s82\n"
    "v_accvgpr_write_b32 a120, v20\n"
    "v_accvgpr_write_b32 a121, v21\n"
    "v_accvgpr_write_b32 a122, v22\n"
    "v_accvgpr_write_b32 a123, v23\n"
    "s_waitcnt vmcnt(0)\n"
    "v_perm_b32 v20, v76, v68, s81\n"
    "v_perm_b32 v21, v92, v84, s81\n"
    "v_perm_b32 v22, v108, v100, s81\n"
    "v_perm_b32 v23, v124, v116, s81\n"
    "v_accvgpr_write_b32 a68, v20\n"
    "v_accvgpr_write_b32 a69, v21\n"
    "v_accvgpr_write_b32 a70, v22\n"
    "v_accvgpr_write_b32 a71, v23\n"
    "v_perm_b32 v20, v76, v68, s82\n"
    "v_perm_b32 v21, v92, v84, s82\n"
    "v_perm_b32 v22, v108, v100, s82\n"
    "v_perm_b32 v23, v124, v116, s82\n"
    "v_accvgpr_write_b32 a76, v20\n"
    "v_accvgpr_write_b32 a77, v21\n"
    "v_accvgpr_write_b32 a78, v22\n"
    "v_accvgpr_write_b32 a79, v23\n"
    "v_perm_b32 v20, v77, v69, s81\n"
    "v_perm_b32 v21, v93, v85, s81\n"
    "v_perm_b32 v22, v109, v101, s81\n"
    "v_perm_b32 v23, v125, v117, s81\n"
    "v_accvgpr_write_b32 a84, v20\n"
    "v_accvgpr_write_b32 a85, v21\n"
    "v_accvgpr_write_b32 a86, v22\n"
    "v_accvgpr_write_b32 a87, v23\n"
    "v_perm_b32 v20, v77, v69, s82\n"
    "v_perm_b32 v21, v93, v85, s82\n"
    "v_perm_b32 v22, v109, v101, s82\n"
    "v_perm_b32 v23, v125, v117, s82\n"
    "v_accvgpr_write_b32 a92, v20\n"
    "v_accvgpr_write_b32 a93, v21\n"
    "v_accvgpr_write_b32 a94, v22\n"
    "v_accvgpr_write_b32 a95, v23\n"
    "v_perm_b32 v20, v78, v70, s81\n"
    "v_perm_b32 v21, v94, v86, s81\n"
    "v_perm_b32 v22, v110, v102, s81\n"
    "v_perm_b32 v23, v126, v118, s81\n"
    "v_accvgpr_write_b32 a100, v20\n"
    "v_accvgpr_write_b32 a101, v21\n"
    "v_accvgpr_write_b32 a102, v22\n"
    "v_accvgpr_write_b32 a103, v23\n"
    "v_perm_b32 v20, v78, v70, s82\n"
    "v_perm_b32 v21, v94, v86, s82\n"
    "v_perm_b32 v22, v110, v102, s82\n"
    "v_perm_b32 v23, v126, v118, s82\n"
    "v_accvgpr_write_b32 a108, v20\n"
    "v_accvgpr_write_b32 a109, v21\n"
    "v_accvgpr_write_b32 a110, v22\n"
    "v_accvgpr_write_b32 a111, v23\n"
    "v_perm_b32 v20, v79, v71, s81\n"
    "v_perm_b32 v21, v95, v87, s81\n"
    "v_perm_b32 v22, v111, v103, s81\n"
    "v_perm_b32 v23, v127, v119, s81\n"
    "v_accvgpr_write_b32 a116, v20\n"
    "v_accvgpr_write_b32 a117, v21\n"
    "v_accvgpr_write_b32 a118, v22\n"
    "v_accvgpr_write_b32 a119, v23\n"
    "v_perm_b32 v20, v79, v71, s82\n"
    "v_perm_b32 v21, v95, v87, s82\n"
    "v_perm_b32 v22, v111, v103, s82\n"
    "v_perm_b32 v23, v127, v119, s82\n"
    "v_accvgpr_write_b32 a124, v20\n"
    "v_accvgpr_write_b32 a125, v21\n"
    "v_accvgpr_write_b32 a126, v22\n"
    "v_accvgpr_write_b32 a127, v23\n"
    "s_nop 1\n"
    "Cpdone_%=:\n"
    "s_lshl_b32 s57, s51, 8\n"
    "v_lshl_add_u32 v5, v25, 10, v0\n"
    "v_add_u32 v5, s57, v5\n"
    "v_add_u32 v6, 20480, v5\n"
    "v_mov_b32 v27, 0x80000000\n"
    "v_cmp_gt_u32 vcc, 2, v25\n"
    "s_nop 1\n"
    "v_cndmask_b32 v6, v27, v6, vcc\n"
    "v_mul_u32_u24 v3, 384, v25\n"
    "v_add_u32 v1, v3, v0\n"
    "v_and_b32 v24, 0x70, v0\n"
    "v_lshrrev_b32 v26, 7, v0\n"
    "v_lshl_add_u32 v24, v26, 3, v24\n"
    "v_add_u32 v3, 256, v3\n"
    "v_add_u32 v3, v24, v3\n"
    "v_add_u32 v2, 1536, v1\n"
    "v_cndmask_b32 v2, v27, v2, s[62:63]\n"
    "v_add_u32 v4, 1536, v3\n"
    "v_cndmask_b32 v4, v27, v4, s[62:63]\n"
    "s_and_b32 s56, s50, 28\n"
    "s_add_u32 s56, s56, s51\n"
    "s_mul_i32 s72, s56, 2688\n"
    "s_add_u32 s72, s72, 622592\n"
    "s_mov_b32 s52, 0\n"
    "s_mov_b32 s84, 0\n"
    "s_mov_b32 s54, 0\n"
    "s_mov_b32 s53, 0\n"
    "s_mov_b32 s59, 0\n"
    "s_cmp_lt_u32 s95, 22\n"
    "s_cbranch_scc1 CO_%=\n"
    "Ctop_%=:\n"
    "s_mov_b32 s59, 0\n"
    "s_lshl_b32 s56, s53, 13\n"
    "s_add_u32 s56, s56, 131072\n"
    "s_add_u32 s57, s56, 4096\n"
    "buffer_load_dwordx4 v[40:43], v5, s[40:43], s56 offen sc1\n"
    "buffer_load_dwordx4 v[40:43], v5, s[40:43], s57 offen sc1\n"
    "s_mul_i32 s56, s53, 22528\n"
    "s_add_u32 s56, s56, 262144\n"
    "buffer_load_dwordx4 v[40:43], v6, s[40:43], s56 offen sc1\n"
    "buffer_load_dwordx4 v[40:43], v5, s[40:43], s56 offen sc1\n"
    "s_add_u32 s56, s56, 4096\n"
    "buffer_load_dwordx4 v[40:43], v5, s[40:43], s56 offen sc1\n"
    "s_add_u32 s56, s56, 4096\n"
    "buffer_load_dwordx4 v[40:43], v5, s[40:43], s56 offen sc1\n"
    "s_add_u32 s56, s56, 4096\n"
    "buffer_load_dwordx4 v[40:43], v5, s[40:43], s56 offen sc1\n"
    "s_add_u32 s56, s56, 4096\n"
    "buffer_load_dwordx4 v[40:43], v5, s[40:43], s56 offen sc1\n"
    "s_cmp_ge_u32 s53, 8\n"
    "s_cbranch_scc1 CO_%=\n"
    "s_lshl_b32 s56, s54, 13\n"
    "s_add_u32 s56, s56, 188416\n"
    "s_add_u32 s57, s56, 4096\n"
    "buffer_load_dwordx4 v[40:43], v5, s[40:43], s56 offen sc1\n"
    "buffer_load_dwordx4 v[40:43], v5, s[40:43], s57 offen sc1\n"
    "s_lshl_b32 s56, s54, 10\n"
    "buffer_load_dword v18, v7, s[44:47], s56 offen\n"
    "CO_%=:\n"
    "s_add_u32 s94, s53, 1\n"
    "s_mul_i32 s94, s94, 0x9e3779b1\n"
    "s_add_u32 s94, s94, s92\n"
    "s_lshr_b32 s58, s53, 2\n"
    "s_and_b32 s58, s58, 2\n"
    "s_and_b32 s57, s53, 1\n"
    "s_add_u32 s57, s57, s58\n"
    "s_mul_i32 s57, s57, 86016\n"
    "s_add_u32 s57, s57, s72\n"
    "Cop_%=:\n"
    "buffer_load_dwordx2 v[24:25], v3, s[40:43], s57 offen sc1\n"
    "buffer_load_dwordx2 v[26:27], v4, s[40:43], s57 offen sc1\n"
    "buffer_load_dwordx4 v[32:35], v1, s[40:43], s57 offen sc1\n"
    "buffer_load_dwordx4 v[36:39], v2, s[40:43], s57 offen sc1\n"
    "s_waitcnt vmcnt(0)\n"
    "Coc_%=:\n"
    "v_mov_b32 v46, s69\n"
    "v_add3_u32 v53, v36, v37, v38\n"
    "v_add3_u32 v54, v39, v26, s94\n"
    "v_cndmask_b32 v26, v46, v26, s[62:63]\n"
    "v_max_f32 v45, v24, v26\n"
    "v_max_f32 v46, v24, v26\n"
    "v_add3_u32 v49, v32, v33, v34\n"
    "v_add3_u32 v50, v35, v24, s94\n"
    "v_permlane16_swap_b32 v45, v46\n"
    "v_max_f32 v45, v45, v46\n"
    "v_mov_b32 v46, v45\n"
    "v_add_u32 v53, v53, v54\n"
    "v_add_u32 v49, v49, v50\n"
    "v_permlane32_swap_b32 v45, v46\n"
    "v_cmp_ne_u32 s[66:67], v53, v27\n"
    "v_cmp_ne_u32 vcc, v49, v25\n"
    "v_max_f32 v45, v45, v46\n"
    "s_and_b64 s[66:67], s[66:67], s[62:63]\n"
    "s_or_b64 vcc, vcc, s[66:67]\n"
    "s_cbranch_scc0 Cod_%=\n"
    "s_sub_u32 s55, s55, 1\n"
    "s_cmp_gt_i32 s55, 0\n"
    "s_cbranch_scc1 Cop_%=\n"
    "Cod_%=:\n"
    "v_sub_f32 v24, v24, v45\n"
    "v_sub_f32 v25, v26, v45\n"
    "v_exp_f32 v24, v24\n"
    "v_exp_f32 v25, v25\n"
    "v_cvt_pk_bf16_f32 v24, v24, v24\n"
    "v_cvt_pk_bf16_f32 v25, v25, v25\n"
    "v_and_b32 v28, v24, v8\n"
    "v_and_b32 v29, v24, v9\n"
    "v_and_b32 v30, v24, v10\n"
    "v_and_b32 v31, v24, v11\n"
    "v_and_b32 v20, v25, v8\n"
    "v_and_b32 v21, v25, v9\n"
    "v_and_b32 v22, v25, v10\n"
    "v_and_b32 v23, v25, v11\n"
    "v_mfma_f32_16x16x32_bf16 v[40:43], v[28:31], v[32:35], 0\n"
    "v_and_b32 v24, s68, v24\n"
    "v_mfma_f32_16x16x32_bf16 v[40:43], v[20:23], v[36:39], v[40:43]\n"
    "v_and_b32 v25, s68, v25\n"
    "v_add_f32 v26, v24, v25\n"
    "v_add_f32 v27, v24, v25\n"
    "v_mov_b32 v32, 0\n"
    "v_mov_b32 v48, 0\n"
    "v_permlane16_swap_b32 v26, v27\n"
    "v_add_f32 v24, v26, v27\n"
    "v_add_f32 v25, v26, v27\n"
    "v_mov_b32 v36, 0\n"
    "v_mov_b32 v52, 0\n"
    "v_permlane32_swap_b32 v24, v25\n"
    "v_add_f32 v24, v24, v25\n"
    "v_rcp_f32 v24, v24\n"
    "v_mov_b32 v56, 0\n"
    "v_mul_f32 v25, v24, v41\n"
    "v_mul_f32 v24, v24, v40\n"
    "v_cvt_pk_bf16_f32 v24, v24, v25\n"
    "v_mov_b32 v44, 0\n"
    "v_mov_b32 v60, 0\n"
    "v_mov_b32_dpp v28, v24 row_newbcast:0 row_mask:0xf bank_mask:0x5\n"
    "v_mov_b32_dpp v29, v24 row_newbcast:2 row_mask:0xf bank_mask:0x5\n"
    "v_mov_b32_dpp v30, v24 row_newbcast:4 row_mask:0xf bank_mask:0x5\n"
    "v_mov_b32_dpp v31, v24 row_newbcast:6 row_mask:0xf bank_mask:0x5\n"
    "v_mov_b32_dpp v28, v24 row_newbcast:1 row_mask:0xf bank_mask:0xa\n"
    "v_mov_b32_dpp v29, v24 row_newbcast:3 row_mask:0xf bank_mask:0xa\n"
    "v_mov_b32_dpp v30, v24 row_newbcast:5 row_mask:0xf bank_mask:0xa\n"
    "v_mov_b32_dpp v31, v24 row_newbcast:7 row_mask:0xf bank_mask:0xa\n"
    "v_mov_b32 v40, 0\n"
    "s_nop 0\n"
    "v_smfmac_f32_16x16x64_bf16 v[32:35], v[28:31], a[0:7], v19\n"
    "v_mov_b32_dpp v20, v24 row_newbcast:8 row_mask:0xf bank_mask:0x5\n"
    "v_mov_b32_dpp v21, v24 row_newbcast:10 row_mask:0xf bank_mask:0x5\n"
    "v_smfmac_f32_16x16x64_bf16 v[48:51], v[28:31], a[32:39], v19\n"
    "v_mov_b32_dpp v22, v24 row_newbcast:12 row_mask:0xf bank_mask:0x5\n"
    "v_mov_b32_dpp v23, v24 row_newbcast:14 row_mask:0xf bank_mask:0x5\n"
    "v_smfmac_f32_16x16x64_bf16 v[36:39], v[28:31], a[8:15], v19\n"
    "v_mov_b32_dpp v20, v24 row_newbcast:9 row_mask:0xf bank_mask:0xa\n"
    "v_mov_b32_dpp v21, v24 row_newbcast:11 row_mask:0xf bank_mask:0xa\n"
    "v_smfmac_f32_16x16x64_bf16 v[52:55], v[28:31], a[40:47], v19\n"
    "v_mov_b32_dpp v22, v24 row_newbcast:13 row_mask:0xf bank_mask:0xa\n"
    "v_mov_b32_dpp v23, v24 row_newbcast:15 row_mask:0xf bank_mask:0xa\n"
    "v_smfmac_f32_16x16x64_bf16 v[40:43], v[28:31], a[16:23], v19\n"
    "v_xor_b32 v12, 8192, v12\n"
    "v_smfmac_f32_16x16x64_bf16 v[56:59], v[28:31], a[48:55], v19\n"
    "v_xor_b32 v13, 8192, v13\n"
    "v_smfmac_f32_16x16x64_bf16 v[44:47], v[28:31], a[24:31], v19\n"
    "v_xor_b32 v14, 8192, v14\n"
    "v_smfmac_f32_16x16x64_bf16 v[60:63], v[28:31], a[56:63], v19\n"
    "s_xor_b32 s84, s84, 8192\n"
    "v_smfmac_f32_16x16x64_bf16 v[32:35], v[20:23], a[64:71], v19\n"
    "s_lshl_b32 s74, s53, 5\n"
    "v_smfmac_f32_16x16x64_bf16 v[48:51], v[20:23], a[96:103], v19\n"
    "s_add_u32 s74, s74, s50\n"
    "v_smfmac_f32_16x16x64_bf16 v[36:39], v[20:23], a[72:79], v19\n"
    "s_lshl_b32 s74, s74, 8\n"
    "v_smfmac_f32_16x16x64_bf16 v[52:55], v[20:23], a[104:111], v19\n"
    "s_add_u32 s74, s74, 131072\n"
    "v_smfmac_f32_16x16x64_bf16 v[40:43], v[20:23], a[80:87], v19\n"
    "v_smfmac_f32_16x16x64_bf16 v[56:59], v[20:23], a[112:119], v19\n"
    "v_smfmac_f32_16x16x64_bf16 v[44:47], v[20:23], a[88:95], v19\n"
    "v_smfmac_f32_16x16x64_bf16 v[60:63], v[20:23], a[120:127], v19\n"
    "v_permlane16_swap_b32 v32, v48\n"
    "v_add_f32 v28, v32, v48\n"
    "s_nop 0\n"
    "v_permlane16_swap_b32 v36, v52\n"
    "v_add_f32 v29, v36, v52\n"
    "s_nop 0\n"
    "v_permlane16_swap_b32 v40, v56\n"
    "v_add_f32 v30, v40, v56\n"
    "v_permlane16_swap_b32 v44, v60\n"
    "v_add_f32 v31, v44, v60\n"
    "s_mov_b32 exec_hi, 0\n"
    "ds_write_b128 v12, v[28:31] offset:0\n"
    "s_mov_b32 exec_hi, -1\n"
    "s_cmp_lg_u32 s51, 0\n"
    "s_waitcnt lgkmcnt(0)\n"
    "s_barrier\n"
    "s_cbranch_scc1 Cyx_%=\n"
    "ds_read2st64_b64 v[32:35], v13 offset0:0 offset1:1\n"
    "ds_read2st64_b64 v[36:39], v13 offset0:2 offset1:3\n"
    "s_cmp_lg_u32 s59, 0\n"
    "s_waitcnt lgkmcnt(1)\n"
    "v_pk_add_f32 v[32:33], v[32:33], v[34:35]\n"
    "s_waitcnt lgkmcnt(0)\n"
    "v_pk_add_f32 v[36:37], v[36:37], v[38:39]\n"
    "v_pk_add_f32 v[32:33], v[32:33], v[36:37]\n"
    "v_cvt_pk_bf16_f32 v29, v32, v33\n"
    "v_min_u32 v28, -2, v29\n"
    "v_min_u32 v29, -2, v29\n"
    "s_nop 1\n"
    "v_permlane16_swap_b32 v28, v29\n"
    "v_mov_b32 v30, v28\n"
    "v_mov_b32 v31, v29\n"
    "s_nop 0\n"
    "v_permlane32_swap_b32 v28, v30\n"
    "v_permlane32_swap_b32 v29, v31\n"
    "s_cbranch_scc1 Cyx_%=\n"
    "s_mov_b64 exec, 0xffff\n"
    "buffer_store_dwordx4 v[28:31], v0, s[40:43], s74 offen sc1\n"
    "s_mov_b64 exec, -1\n"
    "s_nop 0\n"
    "Cyx_%=:\n"
    "s_cmp_lg_u32 s59, 0\n"
    "s_cbranch_scc1 CQ_%=\n"
    "s_cmp_eq_u32 s53, 0\n"
    "s_cbranch_scc1 Cfin_%=\n"
    "s_cmp_lt_u32 s53, 8\n"
    "s_cbranch_scc1 CF_%=\n"
    "s_mov_b32 s56, 14\n"
    "s_cmp_eq_u32 s95, 30\n"
    "s_cbranch_scc1 Crow_%=\n"
    "CF2_%=:\n"
    "s_cmp_lt_u32 s95, 22\n"
    "s_cbranch_scc1 CF_%=\n"
    "s_add_u32 s56, s53, 16\n"
    "s_cmp_lg_u32 s56, s95\n"
    "s_cbranch_scc1 Cnext_%=\n"
    "CF_%=:\n"
    "s_lshl_b32 s73, s53, 13\n"
    "s_add_u32 s73, s73, 131072\n"
    "s_add_u32 s76, s73, 4096\n"
    "v_and_b32 v20, s75, v8\n"
    "v_and_b32 v21, s75, v9\n"
    "v_and_b32 v22, s75, v10\n"
    "v_and_b32 v23, s75, v11\n"
    "s_add_u32 s56, s54, 7\n"
    "s_add_u32 s94, s56, 1\n"
    "s_mul_i32 s94, s94, 0x9e3779b1\n"
    "s_add_u32 s94, s94, s92\n"
    "s_lshr_b32 s58, s56, 2\n"
    "s_and_b32 s58, s58, 2\n"
    "s_and_b32 s57, s56, 1\n"
    "s_add_u32 s57, s57, s58\n"
    "s_mul_i32 s57, s57, 86016\n"
    "s_add_u32 s57, s57, s72\n"
    "s_mul_i32 s56, s53, 22\n"
    "s_add_u32 s56, s56, s95\n"
    "s_lshl_b32 s56, s56, 10\n"
    "s_add_u32 s56, s56, 262144\n"
    "Cyp_%=:\n"
    "buffer_load_dwordx4 v[32:35], v5, s[40:43], s73 offen sc1\n"
    "buffer_load_dwordx4 v[36:39], v5, s[40:43], s76 offen sc1\n"
    "s_waitcnt vmcnt(0)\n"
    "Cyc_%=:\n"
    "v_mfma_f32_16x16x32_bf16 v[40:43], v[20:23], v[32:35], 0\n"
    "v_mfma_f32_16x16x32_bf16 v[40:43], v[20:23], v[36:39], v[40:43]\n"
    "v_max3_u32 v27, v32, v33, v34\n"
    "v_max3_u32 v27, v27, v35, v36\n"
    "v_max3_u32 v27, v27, v37, v38\n"
    "v_max_u32 v27, v27, v39\n"
    "v_cmp_eq_u32 vcc, -1, v27\n"
    "s_cmp_lg_u64 vcc, 0\n"
    "s_cbranch_scc0 Cyd_%=\n"
    "s_sub_u32 s55, s55, 1\n"
    "s_cmp_gt_i32 s55, 0\n"
    "s_nop 0\n"
    "s_cbranch_scc1 Cyp_%=\n"
    "Cyd_%=:\n"
    "s_cmp_ge_u32 s95, 22\n"
    "v_lshlrev_b32 v24, 16, v18\n"
    "v_and_b32 v25, s68, v18\n"
    "v_cvt_pk_bf16_f32 v26, v40, v41\n"
    "v_lshlrev_b32 v27, 16, v26\n"
    "v_and_b32 v26, s68, v26\n"
    "v_add_f32 v24, v24, v27\n"
    "v_add_f32 v25, v25, v26\n"
    "v_cvt_pk_bf16_f32 v18, v24, v25\n"
    "s_cbranch_scc1 Cnf_%=\n"
    "v_lshlrev_b32 v24, 16, v18\n"
    "v_and_b32 v25, s68, v18\n"
    "v_readfirstlane_b32 s71, v24\n"
    "v_mov_b32 v26, s84\n"
    "v_add_u32 v27, s60, v26\n"
    "v_subrev_f32 v34, s71, v24\n"
    "v_subrev_f32 v35, s71, v25\n"
    "v_add_f32 v38, v34, v35\n"
    "v_mul_f32 v39, v34, v34\n"
    "v_fmac_f32 v39, v35, v35\n"
    "v_add_f32_dpp v38, v38, v38 row_ror:8 row_mask:0xf bank_mask:0xf\n"
    "v_mov_b32 v32, 0\n"
    "v_add_f32_dpp v39, v39, v39 row_ror:8 row_mask:0xf bank_mask:0xf\n"
    "v_add_f32_dpp v38, v38, v38 row_ror:4 row_mask:0xf bank_mask:0xf\n"
    "v_mov_b32 v36, 0\n"
    "v_add_f32_dpp v39, v39, v39 row_ror:4 row_mask:0xf bank_mask:0xf\n"
    "v_add_f32_dpp v38, v38, v38 row_ror:2 row_mask:0xf bank_mask:0xf\n"
    "v_mov_b32 v40, 0\n"
    "v_add_f32_dpp v39, v39, v39 row_ror:2 row_mask:0xf bank_mask:0xf\n"
    "v_add_f32_dpp v38, v38, v38 row_ror:1 row_mask:0xf bank_mask:0xf\n"
    "v_mov_b32 v44, 0\n"
    "v_add_f32_dpp v39, v39, v39 row_ror:1 row_mask:0xf bank_mask:0xf\n"
    "v_mov_b32 v48, 0\n"
    "v_mov_b32 v52, 0\n"
    "v_permlane16_swap_b32 v38, v39\n"
    "v_add_f32 v34, v38, v39\n"
    "v_add_f32 v35, v38, v39\n"
    "v_mov_b32 v56, 0\n"
    "v_mov_b32 v60, 0\n"
    "v_permlane32_swap_b32 v34, v35\n"
    "v_add_f32 v38, v34, v35\n"
    "v_add_f32 v39, v34, v35\n"
    "s_nop 1\n"
    "v_permlane16_swap_b32 v38, v39\n"
    "v_mul_f32 v35, 0x3c000000, v38\n"
    "s_mov_b64 exec, 1\n"
    "v_add_f32 v34, s71, v35\n"
    "v_fma_f32 v35, -v38, v35, v39\n"
    "ds_write_b64 v27, v[34:35] offset:6144\n"
    "s_mov_b64 exec, 0x1fffff\n"
    "v_and_b32 v27, 0xfc, v14\n"
    "v_lshlrev_b32 v27, 5, v27\n"
    "buffer_load_dword v17, v27, s[40:43], s57 offen sc1\n"
    "s_mov_b64 exec, -1\n"
    "s_waitcnt lgkmcnt(0)\n"
    "s_barrier\n"
    "ds_read_b128 v[20:23], v26 offset:6144\n"
    "ds_read_b128 v[28:31], v26 offset:6160\n"
    "s_waitcnt lgkmcnt(1)\n"
    "v_pk_add_f32 v[34:35], v[20:21], v[22:23]\n"
    "s_waitcnt lgkmcnt(0)\n"
    "v_pk_add_f32 v[38:39], v[28:29], v[30:31]\n"
    "v_pk_add_f32 v[34:35], v[34:35], v[38:39]\n"
    "v_mul_f32 v34, 0x3e800000, v34\n"
    "v_mul_f32 v35, 0x3c000000, v35\n"
    "v_sub_f32 v20, v20, v34\n"
    "v_sub_f32 v22, v22, v34\n"
    "v_sub_f32 v28, v28, v34\n"
    "v_sub_f32 v30, v30, v34\n"
    "v_fmac_f32 v35, v20, v20\n"
    "v_fmac_f32 v35, v22, v22\n"
    "v_fmac_f32 v35, v28, v28\n"
    "v_fmac_f32 v35, v30, v30\n"
    "v_mul_f32 v35, 0x3e800000, v35\n"
    "v_cvt_pk_bf16_f32 v26, v34, v35\n"
    "v_and_b32 v35, s68, v26\n"
    "v_add_f32 v35, 0x3727c5ac, v35\n"
    "v_cvt_pk_bf16_f32 v35, v35, v35\n"
    "v_and_b32 v35, s68, v35\n"
    "v_sqrt_f32 v35, v35\n"
    "v_lshlrev_b32 v34, 16, v26\n"
    "v_cvt_pk_bf16_f32 v35, v35, v35\n"
    "v_and_b32 v35, s68, v35\n"
    "v_rcp_f32 v35, v35\n"
    "v_sub_f32 v24, v24, v34\n"
    "v_sub_f32 v25, v25, v34\n"
    "v_cvt_pk_bf16_f32 v25, v24, v25\n"
    "v_lshlrev_b32 v24, 16, v25\n"
    "v_and_b32 v25, s68, v25\n"
    "v_mul_f32 v24, v35, v24\n"
    "v_mul_f32 v25, v35, v25\n"
    "v_cvt_pk_bf16_f32 v24, v24, v25\n"
    "s_nop 1\n"
    "v_mov_b32_dpp v28, v24 row_newbcast:0 row_mask:0xf bank_mask:0x5\n"
    "v_mov_b32_dpp v29, v24 row_newbcast:2 row_mask:0xf bank_mask:0x5\n"
    "v_mov_b32_dpp v30, v24 row_newbcast:4 row_mask:0xf bank_mask:0x5\n"
    "v_mov_b32_dpp v31, v24 row_newbcast:6 row_mask:0xf bank_mask:0x5\n"
    "v_mov_b32_dpp v28, v24 row_newbcast:1 row_mask:0xf bank_mask:0xa\n"
    "v_mov_b32_dpp v29, v24 row_newbcast:3 row_mask:0xf bank_mask:0xa\n"
    "v_mov_b32_dpp v30, v24 row_newbcast:5 row_mask:0xf bank_mask:0xa\n"
    "v_mov_b32_dpp v31, v24 row_newbcast:7 row_mask:0xf bank_mask:0xa\n"
    "s_nop 1\n"
    "v_smfmac_f32_16x16x64_bf16 v[32:35], v[28:31], v[128:135], v19\n"
    "v_mov_b32_dpp v20, v24 row_newbcast:8 row_mask:0xf bank_mask:0x5\n"
    "v_mov_b32_dpp v21, v24 row_newbcast:10 row_mask:0xf bank_mask:0x5\n"
    "v_smfmac_f32_16x16x64_bf16 v[48:51], v[28:31], v[160:167], v19\n"
    "v_mov_b32_dpp v22, v24 row_newbcast:12 row_mask:0xf bank_mask:0x5\n"
    "v_mov_b32_dpp v23, v24 row_newbcast:14 row_mask:0xf bank_mask:0x5\n"
    "v_smfmac_f32_16x16x64_bf16 v[36:39], v[28:31], v[136:143], v19\n"
    "v_mov_b32_dpp v20, v24 row_newbcast:9 row_mask:0xf bank_mask:0xa\n"
    "v_mov_b32_dpp v21, v24 row_newbcast:11 row_mask:0xf bank_mask:0xa\n"
    "v_smfmac_f32_16x16x64_bf16 v[52:55], v[28:31], v[168:175], v19\n"
    "v_mov_b32_dpp v22, v24 row_newbcast:13 row_mask:0xf bank_mask:0xa\n"
    "v_mov_b32_dpp v23, v24 row_newbcast:15 row_mask:0xf bank_mask:0xa\n"
    "v_smfmac_f32_16x16x64_bf16 v[40:43], v[28:31], v[144:151], v19\n"
    "v_smfmac_f32_16x16x64_bf16 v[56:59], v[28:31], v[176:183], v19\n"
    "v_smfmac_f32_16x16x64_bf16 v[44:47], v[28:31], v[152:159], v19\n"
    "v_smfmac_f32_16x16x64_bf16 v[60:63], v[28:31], v[184:191], v19\n"
    "v_smfmac_f32_16x16x64_bf16 v[32:35], v[20:23], v[192:199], v19\n"
    "v_smfmac_f32_16x16x64_bf16 v[48:51], v[20:23], v[224:231], v19\n"
    "v_smfmac_f32_16x16x64_bf16 v[36:39], v[20:23], v[200:207], v19\n"
    "v_smfmac_f32_16x16x64_bf16 v[52:55], v[20:23], v[232:239], v19\n"
    "v_smfmac_f32_16x16x64_bf16 v[40:43], v[20:23], v[208:215], v19\n"
    "v_smfmac_f32_16x16x64_bf16 v[56:59], v[20:23], v[240:247], v19\n"
    "v_smfmac_f32_16x16x64_bf16 v[44:47], v[20:23], v[216:223], v19\n"
    "v_smfmac_f32_16x16x64_bf16 v[60:63], v[20:23], v[248:255], v19\n"
    "s_nop 1\n"
    "v_permlane16_swap_b32 v32, v48\n"
    "v_add_f32 v28, v32, v48\n"
    "v_permlane16_swap_b32 v36, v52\n"
    "v_add_f32 v29, v36, v52\n"
    "v_permlane16_swap_b32 v40, v56\n"
    "v_add_f32 v30, v40, v56\n"
    "v_permlane16_swap_b32 v44, v60\n"
    "v_add_f32 v31, v44, v60\n"
    "s_mov_b32 exec_hi, 0\n"
    "ds_write_b128 v12, v[28:31] offset:2048\n"
    "s_mov_b32 exec_hi, -1\n"
    "s_cmp_lg_u32 s51, 0\n"
    "s_waitcnt lgkmcnt(0)\n"
    "s_barrier\n"
    "ds_read2st64_b32 v[34:35], v14 offset0:8 offset1:9\n"
    "ds_read2st64_b32 v[38:39], v14 offset0:10 offset1:11\n"
    "ds_read2st64_b32 v[42:43], v14 offset0:12 offset1:13\n"
    "ds_read2st64_b32 v[46:47], v14 offset0:14 offset1:15\n"
    "v_mov_b32 v32, 0\n"
    "v_mov_b32 v36, 0\n"
    "v_mov_b32 v40, 0\n"
    "v_mov_b32 v44, 0\n"
    "v_mov_b32 v48, 0\n"
    "v_mov_b32 v52, 0\n"
    "v_mov_b32 v56, 0\n"
    "v_mov_b32 v60, 0\n"
    "s_waitcnt lgkmcnt(2)\n"
    "v_pk_add_f32 v[34:35], v[34:35], v[38:39]\n"
    "s_waitcnt lgkmcnt(0)\n"
    "v_pk_add_f32 v[42:43], v[42:43], v[46:47]\n"
    "v_pk_add_f32 v[34:35], v[34:35], v[42:43]\n"
    "v_cvt_pk_bf16_f32 v25, v34, v35\n"
    "v_lshlrev_b32 v24, 16, v25\n"
    "v_mul_f32 v24, s70, v24\n"
    "v_exp_f32 v24, v24\n"
    "v_and_b32 v25, s68, v25\n"
    "v_add_f32 v24, 1.0, v24\n"
    "v_rcp_f32 v24, v24\n"
    "v_cndmask_b32 v25, 0, v25, s[64:65]\n"
    "v_cvt_pk_bf16_f32 v24, v24, v24\n"
    "v_and_b32 v24, s68, v24\n"
    "v_mul_f32 v24, v24, v25\n"
    "s_nop 1\n"
    "v_mov_b32_dpp v25, v24 quad_perm:[1,1,3,3] row_mask:0xf bank_mask:0xf\n"
    "v_cvt_pk_bf16_f32 v24, v24, v25\n"
    "s_nop 1\n"
    "v_mov_b32_dpp v28, v24 row_newbcast:0 row_mask:0xf bank_mask:0x5\n"
    "v_mov_b32_dpp v29, v24 row_newbcast:4 row_mask:0xf bank_mask:0x5\n"
    "v_mov_b32_dpp v30, v24 row_newbcast:8 row_mask:0xf bank_mask:0x5\n"
    "v_mov_b32_dpp v31, v24 row_newbcast:12 row_mask:0xf bank_mask:0x5\n"
    "v_mov_b32_dpp v28, v24 row_newbcast:2 row_mask:0xf bank_mask:0xa\n"
    "v_mov_b32_dpp v29, v24 row_newbcast:6 row_mask:0xf bank_mask:0xa\n"
    "v_mov_b32_dpp v30, v24 row_newbcast:10 row_mask:0xf bank_mask:0xa\n"
    "v_mov_b32_dpp v31, v24 row_newbcast:14 row_mask:0xf bank_mask:0xa\n"
    "s_nop 1\n"
    "v_smfmac_f32_16x16x64_bf16 v[32:35], v[28:31], v[64:71], v19\n"
    "v_smfmac_f32_16x16x64_bf16 v[48:51], v[28:31], v[96:103], v19\n"
    "v_smfmac_f32_16x16x64_bf16 v[36:39], v[28:31], v[72:79], v19\n"
    "v_smfmac_f32_16x16x64_bf16 v[52:55], v[28:31], v[104:111], v19\n"
    "v_smfmac_f32_16x16x64_bf16 v[40:43], v[28:31], v[80:87], v19\n"
    "v_smfmac_f32_16x16x64_bf16 v[56:59], v[28:31], v[112:119], v19\n"
    "v_smfmac_f32_16x16x64_bf16 v[44:47], v[28:31], v[88:95], v19\n"
    "v_smfmac_f32_16x16x64_bf16 v[60:63], v[28:31], v[120:127], v19\n"
    "buffer_load_dwordx2 v[24:25], v3, s[40:43], s57 offen sc1\n"
    "buffer_load_dwordx2 v[26:27], v4, s[40:43], s57 offen sc1\n"
    "v_permlane16_swap_b32 v32, v48\n"
    "v_add_f32 v20, v32, v48\n"
    "v_permlane16_swap_b32 v36, v52\n"
    "v_add_f32 v21, v36, v52\n"
    "v_permlane16_swap_b32 v40, v56\n"
    "v_add_f32 v22, v40, v56\n"
    "v_permlane16_swap_b32 v44, v60\n"
    "v_add_f32 v23, v44, v60\n"
    "buffer_load_dwordx4 v[32:35], v1, s[40:43], s57 offen sc1\n"
    "buffer_load_dwordx4 v[36:39], v2, s[40:43], s57 offen sc1\n"
    "v_cvt_pk_bf16_f32 v30, v20, v21\n"
    "v_cvt_pk_bf16_f32 v31, v22, v23\n"
    "v_min_u32 v28, -2, v30\n"
    "v_min_u32 v30, -2, v30\n"
    "v_min_u32 v29, -2, v31\n"
    "v_min_u32 v31, -2, v31\n"
    "v_permlane16_swap_b32 v28, v30\n"
    "s_nop 0\n"
    "v_permlane16_swap_b32 v29, v31\n"
    "s_mov_b64 exec, 0xffff\n"
    "buffer_store_dwordx4 v[28:31], v5, s[40:43], s56 offen sc1\n"
    "s_mov_b64 exec, -1\n"
    "s_waitcnt vmcnt(1)\n"
    "s_cmp_ge_u32 s53, 8\n"
    "s_cbranch_scc1 Cnext_%=\n"
    "s_cmp_eq_u32 s54, 0\n"
    "s_cbranch_scc1 CQ_%=\n"
    "s_add_u32 s53, s54, 7\n"
    "s_mov_b32 s59, 1\n"
    "s_branch Coc_%=\n"
    "Cfin_%=:\n"
    "s_cmp_ge_u32 s95, 22\n"
    "s_cbranch_scc1 CF_%=\n"
    "v_perm_b32 v20, v204, v196, s81\n"
    "v_perm_b32 v204, v204, v196, s82\n"
    "v_mov_b32 v196, v20\n"
    "v_perm_b32 v20, v205, v197, s81\n"
    "v_perm_b32 v21, v205, v197, s82\n"
    "v_perm_b32 v197, v220, v212, s81\n"
    "v_perm_b32 v205, v220, v212, s82\n"
    "v_mov_b32 v212, v20\n"
    "v_mov_b32 v220, v21\n"
    "v_perm_b32 v20, v206, v198, s81\n"
    "v_perm_b32 v21, v206, v198, s82\n"
    "v_perm_b32 v198, v236, v228, s81\n"
    "v_perm_b32 v206, v236, v228, s82\n"
    "v_mov_b32 v228, v20\n"
    "v_mov_b32 v236, v21\n"
    "v_perm_b32 v20, v207, v199, s81\n"
    "v_perm_b32 v21, v207, v199, s82\n"
    "v_perm_b32 v199, v252, v244, s81\n"
    "v_perm_b32 v207, v252, v244, s82\n"
    "v_mov_b32 v244, v20\n"
    "v_mov_b32 v252, v21\n"
    "v_perm_b32 v20, v221, v213, s81\n"
    "v_perm_b32 v221, v221, v213, s82\n"
    "v_mov_b32 v213, v20\n"
    "v_perm_b32 v20, v222, v214, s81\n"
    "v_perm_b32 v21, v222, v214, s82\n"
    "v_perm_b32 v214, v237, v229, s81\n"
    "v_perm_b32 v222, v237, v229, s82\n"
    "v_mov_b32 v229, v20\n"
    "v_mov_b32 v237, v21\n"
    "v_perm_b32 v20, v223, v215, s81\n"
    "v_perm_b32 v21, v223, v215, s82\n"
    "v_perm_b32 v215, v253, v245, s81\n"
    "v_perm_b32 v223, v253, v245, s82\n"
    "v_mov_b32 v245, v20\n"
    "v_mov_b32 v253, v21\n"
    "v_perm_b32 v20, v238, v230, s81\n"
    "v_perm_b32 v238, v238, v230, s82\n"
    "v_mov_b32 v230, v20\n"
    "v_perm_b32 v20, v239, v231, s81\n"
    "v_perm_b32 v21, v239, v231, s82\n"
    "v_perm_b32 v231, v254, v246, s81\n"
    "v_perm_b32 v239, v254, v246, s82\n"
    "v_mov_b32 v246, v20\n"
    "v_mov_b32 v254, v21\n"
    "v_perm_b32 v20, v255, v247, s81\n"
    "v_perm_b32 v255, v255, v247, s82\n"
    "v_mov_b32 v247, v20\n"
    "v_perm_b32 v20, v72, v64, s81\n"
    "v_perm_b32 v72, v72, v64, s82\n"
    "v_mov_b32 v64, v20\n"
    "v_perm_b32 v20, v73, v65, s81\n"
    "v_perm_b32 v21, v73, v65, s82\n"
    "v_perm_b32 v65, v88, v80, s81\n"
    "v_perm_b32 v73, v88, v80, s82\n"
    "v_mov_b32 v80, v20\n"
    "v_mov_b32 v88, v21\n"
    "v_perm_b32 v20, v74, v66, s81\n"
    "v_perm_b32 v21, v74, v66, s82\n"
    "v_perm_b32 v66, v104, v96, s81\n"
    "v_perm_b32 v74, v104, v96, s82\n"
    "v_mov_b32 v96, v20\n"
    "v_mov_b32 v104, v21\n"
    "v_perm_b32 v20, v75, v67, s81\n"
    "v_perm_b32 v21, v75, v67, s82\n"
    "v_perm_b32 v67, v120, v112, s81\n"
    "v_perm_b32 v75, v120, v112, s82\n"
    "v_mov_b32 v112, v20\n"
    "v_mov_b32 v120, v21\n"
    "v_perm_b32 v20, v89, v81, s81\n"
    "v_perm_b32 v89, v89, v81, s82\n"
    "v_mov_b32 v81, v20\n"
    "v_perm_b32 v20, v90, v82, s81\n"
    "v_perm_b32 v21, v90, v82, s82\n"
    "v_perm_b32 v82, v105, v97, s81\n"
    "v_perm_b32 v90, v105, v97, s82\n"
    "v_mov_b32 v97, v20\n"
    "v_mov_b32 v105, v21\n"
    "v_perm_b32 v20, v91, v83, s81\n"
    "v_perm_b32 v21, v91, v83, s82\n"
    "v_perm_b32 v83, v121, v113, s81\n"
    "v_perm_b32 v91, v121, v113, s82\n"
    "v_mov_b32 v113, v20\n"
    "v_mov_b32 v121, v21\n"
    "v_perm_b32 v20, v106, v98, s81\n"
    "v_perm_b32 v106, v106, v98, s82\n"
    "v_mov_b32 v98, v20\n"
    "v_perm_b32 v20, v107, v99, s81\n"
    "v_perm_b32 v21, v107, v99, s82\n"
    "v_perm_b32 v99, v122, v114, s81\n"
    "v_perm_b32 v107, v122, v114, s82\n"
    "v_mov_b32 v114, v20\n"
    "v_mov_b32 v122, v21\n"
    "v_perm_b32 v20, v123, v115, s81\n"
    "v_perm_b32 v123, v123, v115, s82\n"
    "v_mov_b32 v115, v20\n"
    "v_perm_b32 v20, v76, v68, s81\n"
    "v_perm_b32 v76, v76, v68, s82\n"
    "v_mov_b32 v68, v20\n"
    "v_perm_b32 v20, v77, v69, s81\n"
    "v_perm_b32 v21, v77, v69, s82\n"
    "v_perm_b32 v69, v92, v84, s81\n"
    "v_perm_b32 v77, v92, v84, s82\n"
    "v_mov_b32 v84, v20\n"
    "v_mov_b32 v92, v21\n"
    "v_perm_b32 v20, v78, v70, s81\n"
    "v_perm_b32 v21, v78, v70, s82\n"
    "v_perm_b32 v70, v108, v100, s81\n"
    "v_perm_b32 v78, v108, v100, s82\n"
    "v_mov_b32 v100, v20\n"
    "v_mov_b32 v108, v21\n"
    "v_perm_b32 v20, v79, v71, s81\n"
    "v_perm_b32 v21, v79, v71, s82\n"
    "v_perm_b32 v71, v124, v116, s81\n"
    "v_perm_b32 v79, v124, v116, s82\n"
    "v_mov_b32 v116, v20\n"
    "v_mov_b32 v124, v21\n"
    "v_perm_b32 v20, v93, v85, s81\n"
    "v_perm_b32 v93, v93, v85, s82\n"
    "v_mov_b32 v85, v20\n"
    "v_perm_b32 v20, v94, v86, s81\n"
    "v_perm_b32 v21, v94, v86, s82\n"
    "v_perm_b32 v86, v109, v101, s81\n"
    "v_perm_b32 v94, v109, v101, s82\n"
    "v_mov_b32 v101, v20\n"
    "v_mov_b32 v109, v21\n"
    "v_perm_b32 v20, v95, v87, s81\n"
    "v_perm_b32 v21, v95, v87, s82\n"
    "v_perm_b32 v87, v125, v117, s81\n"
    "v_perm_b32 v95, v125, v117, s82\n"
    "v_mov_b32 v117, v20\n"
    "v_mov_b32 v125, v21\n"
    "v_perm_b32 v20, v110, v102, s81\n"
    "v_perm_b32 v110, v110, v102, s82\n"
    "v_mov_b32 v102, v20\n"
    "v_perm_b32 v20, v111, v103, s81\n"
    "v_perm_b32 v21, v111, v103, s82\n"
    "v_perm_b32 v103, v126, v118, s81\n"
    "v_perm_b32 v111, v126, v118, s82\n"
    "v_mov_b32 v118, v20\n"
    "v_mov_b32 v126, v21\n"
    "v_perm_b32 v20, v127, v119, s81\n"
    "v_perm_b32 v127, v127, v119, s82\n"
    "v_mov_b32 v119, v20\n"
    "s_nop 1\n"
    "s_branch CF_%=\n"
    "Cnf_%=:\n"
    "s_cmp_lt_u32 s53, 8\n"
    "s_cbranch_scc1 Cnf1_%=\n"
    "v_mov_b32 v17, v18\n"
    "s_cmp_lg_u32 s53, 15\n"
    "s_nop 0\n"
    "s_cbranch_scc1 Cnext_%=\n"
    "s_mov_b32 s56, 15\n"
    "Crow_%=:\n"
    "s_sub_u32 s57, s56, 8\n"
    "s_lshl_b32 s57, s57, 10\n"
    "s_add_u32 s66, s48, s57\n"
    "s_addc_u32 s67, s49, 0\n"
    "v_mov_b32 v61, v17\n"
    "s_nop 1\n"
    "s_branch CZ_%=\n"
    "Cnf1_%=:\n"
    "s_lshl_b32 s56, s95, 2\n"
    "s_add_u32 s56, s56, s51\n"
    "s_cmp_lg_u32 s56, 89\n"
    "s_cbranch_scc1 Cnf3_%=\n"
    "s_memrealtime s[86:87]\n"
    "s_lshl_b32 s56, s54, 7\n"
    "s_add_u32 s56, s56, 971008\n"
    "s_waitcnt lgkmcnt(0)\n"
    "v_mov_b32 v32, s86\n"
    "v_mov_b32 v33, s86\n"
    "v_mov_b32 v34, s86\n"
    "v_mov_b32 v35, s86\n"
    "s_mov_b64 exec, 0xff\n"
    "buffer_store_dwordx4 v[32:35], v0, s[40:43], s56 offen sc1\n"
    "s_mov_b64 exec, -1\n"
    "s_nop 0\n"
    "Cnf3_%=:\n"
    "s_add_u32 s56, s54, 22\n"
    "s_cmp_lg_u32 s56, s95\n"
    "s_cbranch_scc1 Cnf2_%=\n"
    "s_cmp_lt_u32 s54, 2\n"
    "s_cbranch_scc1 Cnf2_%=\n"
    "s_add_u32 s56, s54, 6\n"
    "s_branch Crow_%=\n"
    "Cnf2_%=:\n"
    "s_cmp_eq_u32 s54, 0\n"
    "s_cbranch_scc1 CQ_%=\n"
    "s_add_u32 s53, s54, 7\n"
    "s_mov_b32 s59, 1\n"
    "s_branch CO_%=\n"
    "CQ_%=:\n"
    "v_mov_b32 v15, v16\n"
    "s_add_u32 s73, s54, 8\n"
    "s_lshl_b32 s73, s73, 5\n"
    "s_add_u32 s73, s73, s50\n"
    "s_lshl_b32 s73, s73, 8\n"
    "s_mov_b32 s56, s54\n"
    "v_mov_b32 v61, v18\n"
    "s_nop 1\n"
    "CZ_%=:\n"
    "s_mul_i32 s76, s56, 22528\n"
    "s_add_u32 s76, s76, 262144\n"
    "s_add_u32 s77, s76, 4096\n"
    "s_add_u32 s78, s76, 8192\n"
    "s_add_u32 s79, s76, 12288\n"
    "s_add_u32 s80, s76, 16384\n"
    "v_and_b32 v20, s75, v8\n"
    "v_and_b32 v21, s75, v9\n"
    "v_and_b32 v22, s75, v10\n"
    "v_and_b32 v23, s75, v11\n"
    "s_nop 1\n"
    "Czqp_%=:\n"
    "buffer_load_dwordx4 v[32:35], v5, s[40:43], s76 offen sc1\n"
    "buffer_load_dwordx4 v[36:39], v5, s[40:43], s77 offen sc1\n"
    "buffer_load_dwordx4 v[40:43], v5, s[40:43], s78 offen sc1\n"
    "buffer_load_dwordx4 v[44:47], v5, s[40:43], s79 offen sc1\n"
    "buffer_load_dwordx4 v[48:51], v5, s[40:43], s80 offen sc1\n"
    "buffer_load_dwordx4 v[52:55], v6, s[40:43], s76 offen sc1\n"
    "s_waitcnt vmcnt(0)\n"
    "Czqc_%=:\n"
    "v_mfma_f32_16x16x32_bf16 v[56:59], v[20:23], v[32:35], 0\n"
    "v_max3_u32 v27, v32, v33, v34\n"
    "v_max_u32 v27, v27, v35\n"
    "v_mfma_f32_16x16x32_bf16 v[56:59], v[20:23], v[36:39], v[56:59]\n"
    "v_max3_u32 v27, v27, v36, v37\n"
    "v_max3_u32 v27, v27, v38, v39\n"
    "v_mfma_f32_16x16x32_bf16 v[56:59], v[20:23], v[40:43], v[56:59]\n"
    "v_max3_u32 v27, v27, v40, v41\n"
    "v_max3_u32 v27, v27, v42, v43\n"
    "v_mfma_f32_16x16x32_bf16 v[56:59], v[20:23], v[44:47], v[56:59]\n"
    "v_max3_u32 v27, v27, v44, v45\n"
    "v_max3_u32 v27, v27, v46, v47\n"
    "v_mfma_f32_16x16x32_bf16 v[56:59], v[20:23], v[48:51], v[56:59]\n"
    "v_max3_u32 v27, v27, v48, v49\n"
    "v_max3_u32 v27, v27, v50, v51\n"
    "v_mfma_f32_16x16x32_bf16 v[56:59], v[20:23], v[52:55], v[56:59]\n"
    "v_max3_u32 v27, v27, v52, v53\n"
    "v_max3_u32 v27, v27, v54, v55\n"
    "v_cmp_eq_u32 vcc, -1, v27\n"
    "s_cmp_lg_u64 vcc, 0\n"
    "s_cbranch_scc0 Czqd_%=\n"
    "s_sub_u32 s55, s55, 1\n"
    "s_cmp_gt_i32 s55, 0\n"
    "s_nop 0\n"
    "s_cbranch_scc1 Czqp_%=\n"
    "Czqd_%=:\n"
    "s_cmp_lt_u32 s56, 8\n"
    "v_lshlrev_b32 v24, 16, v61\n"
    "v_and_b32 v25, s68, v61\n"
    "v_cvt_pk_bf16_f32 v26, v56, v57\n"
    "v_lshlrev_b32 v27, 16, v26\n"
    "v_and_b32 v26, s68, v26\n"
    "v_add_f32 v24, v24, v27\n"
    "v_add_f32 v25, v25, v26\n"
    "v_cvt_pk_bf16_f32 v61, v24, v25\n"
    "s_cbranch_scc1 CQ2_%=\n"
    "global_store_dword v7, v61, s[66:67]\n"
    "s_cmp_lt_u32 s54, 8\n"
    "s_nop 0\n"
    "s_cbranch_scc1 Cnf2_%=\n"
    "s_cmp_lg_u32 s95, 31\n"
    "s_cbranch_scc1 Cnext_%=\n"
    "s_cmp_lg_u32 s51, 0\n"
    "s_cbranch_scc1 Cnext_%=\n"
    "v_mov_b32 v32, s92\n"
    "v_mov_b32 v33, s93\n"
    "s_mov_b32 s56, 970752\n"
    "s_mov_b64 exec, 1\n"
    "buffer_store_dwordx2 v[32:33], off, s[40:43], s56 sc1\n"
    "s_mov_b64 exec, -1\n"
    "s_nop 0\n"
    "s_branch Cnext_%=\n"
    "CQ2_%=:\n"
    "v_lshlrev_b32 v24, 16, v61\n"
    "v_and_b32 v25, s68, v61\n"
    "v_readfirstlane_b32 s71, v24\n"
    "v_mov_b32 v26, s84\n"
    "v_add_u32 v27, s60, v26\n"
    "v_subrev_f32 v34, s71, v24\n"
    "v_subrev_f32 v35, s71, v25\n"
    "v_add_f32 v38, v34, v35\n"
    "v_mul_f32 v39, v34, v34\n"
    "v_fmac_f32 v39, v35, v35\n"
    "v_add_f32_dpp v38, v38, v38 row_ror:8 row_mask:0xf bank_mask:0xf\n"
    "v_mov_b32 v16, v61\n"
    "v_add_f32_dpp v39, v39, v39 row_ror:8 row_mask:0xf bank_mask:0xf\n"
    "v_add_f32_dpp v38, v38, v38 row_ror:4 row_mask:0xf bank_mask:0xf\n"
    "v_mov_b32 v32, 0\n"
    "v_add_f32_dpp v39, v39, v39 row_ror:4 row_mask:0xf bank_mask:0xf\n"
    "v_add_f32_dpp v38, v38, v38 row_ror:2 row_mask:0xf bank_mask:0xf\n"
    "v_mov_b32 v36, 0\n"
    "v_add_f32_dpp v39, v39, v39 row_ror:2 row_mask:0xf bank_mask:0xf\n"
    "v_add_f32_dpp v38, v38, v38 row_ror:1 row_mask:0xf bank_mask:0xf\n"
    "v_mov_b32 v40, 0\n"
    "v_add_f32_dpp v39, v39, v39 row_ror:1 row_mask:0xf bank_mask:0xf\n"
    "v_mov_b32 v44, 0\n"
    "v_mov_b32 v48, 0\n"
    "v_permlane16_swap_b32 v38, v39\n"
    "v_add_f32 v34, v38, v39\n"
    "v_add_f32 v35, v38, v39\n"
    "v_mov_b32 v52, 0\n"
    "v_mov_b32 v56, 0\n"
    "v_permlane32_swap_b32 v34, v35\n"
    "v_add_f32 v38, v34, v35\n"
    "v_add_f32 v39, v34, v35\n"
    "v_mov_b32 v60, 0\n"
    "s_nop 0\n"
    "v_permlane16_swap_b32 v38, v39\n"
    "v_mul_f32 v35, 0x3c000000, v38\n"
    "s_mov_b64 exec, 1\n"
    "v_add_f32 v34, s71, v35\n"
    "v_fma_f32 v35, -v38, v35, v39\n"
    "ds_write_b64 v27, v[34:35] offset:6176\n"
    "s_cmp_gt_u32 s59, s51\n"
    "s_cbranch_scc0 Cy2x_%=\n"
    "s_mov_b64 exec, 0xffff\n"
    "buffer_store_dwordx4 v[28:31], v0, s[40:43], s74 offen sc1\n"
    "Cy2x_%=:\n"
    "s_mov_b64 exec, -1\n"
    "s_waitcnt lgkmcnt(0)\n"
    "s_barrier\n"
    "ds_read_b128 v[20:23], v26 offset:6176\n"
    "ds_read_b128 v[28:31], v26 offset:6192\n"
    "s_waitcnt lgkmcnt(1)\n"
    "v_pk_add_f32 v[34:35], v[20:21], v[22:23]\n"
    "s_waitcnt lgkmcnt(0)\n"
    "v_pk_add_f32 v[38:39], v[28:29], v[30:31]\n"
    "v_pk_add_f32 v[34:35], v[34:35], v[38:39]\n"
    "v_mul_f32 v34, 0x3e800000, v34\n"
    "v_mul_f32 v35, 0x3c000000, v35\n"
    "v_sub_f32 v20, v20, v34\n"
    "v_sub_f32 v22, v22, v34\n"
    "v_sub_f32 v28, v28, v34\n"
    "v_sub_f32 v30, v30, v34\n"
    "v_fmac_f32 v35, v20, v20\n"
    "v_fmac_f32 v35, v22, v22\n"
    "v_fmac_f32 v35, v28, v28\n"
    "v_fmac_f32 v35, v30, v30\n"
    "v_mul_f32 v35, 0x3e800000, v35\n"
    "v_cvt_pk_bf16_f32 v26, v34, v35\n"
    "v_and_b32 v35, s68, v26\n"
    "v_add_f32 v35, 0x3727c5ac, v35\n"
    "v_cvt_pk_bf16_f32 v35, v35, v35\n"
    "v_and_b32 v35, s68, v35\n"
    "v_sqrt_f32 v35, v35\n"
    "v_lshlrev_b32 v34, 16, v26\n"
    "v_cvt_pk_bf16_f32 v35, v35, v35\n"
    "v_and_b32 v35, s68, v35\n"
    "v_rcp_f32 v35, v35\n"
    "v_sub_f32 v24, v24, v34\n"
    "v_sub_f32 v25, v25, v34\n"
    "v_cvt_pk_bf16_f32 v25, v24, v25\n"
    "v_lshlrev_b32 v24, 16, v25\n"
    "v_and_b32 v25, s68, v25\n"
    "v_mul_f32 v24, v35, v24\n"
    "v_mul_f32 v25, v35, v25\n"
    "v_cvt_pk_bf16_f32 v24, v24, v25\n"
    "s_nop 1\n"
    "v_mov_b32_dpp v28, v24 row_newbcast:0 row_mask:0xf bank_mask:0x5\n"
    "v_mov_b32_dpp v29, v24 row_newbcast:2 row_mask:0xf bank_mask:0x5\n"
    "v_mov_b32_dpp v30, v24 row_newbcast:4 row_mask:0xf bank_mask:0x5\n"
    "v_mov_b32_dpp v31, v24 row_newbcast:6 row_mask:0xf bank_mask:0x5\n"
    "v_mov_b32_dpp v28, v24 row_newbcast:1 row_mask:0xf bank_mask:0xa\n"
    "v_mov_b32_dpp v29, v24 row_newbcast:3 row_mask:0xf bank_mask:0xa\n"
    "v_mov_b32_dpp v30, v24 row_newbcast:5 row_mask:0xf bank_mask:0xa\n"
    "v_mov_b32_dpp v31, v24 row_newbcast:7 row_mask:0xf bank_mask:0xa\n"
    "s_nop 1\n"
    "v_smfmac_f32_16x16x64_bf16 v[32:35], v[28:31], a[128:135], v19\n"
    "v_mov_b32_dpp v20, v24 row_newbcast:8 row_mask:0xf bank_mask:0x5\n"
    "v_mov_b32_dpp v21, v24 row_newbcast:10 row_mask:0xf bank_mask:0x5\n"
    "v_smfmac_f32_16x16x64_bf16 v[48:51], v[28:31], a[160:167], v19\n"
    "v_mov_b32_dpp v22, v24 row_newbcast:12 row_mask:0xf bank_mask:0x5\n"
    "v_mov_b32_dpp v23, v24 row_newbcast:14 row_mask:0xf bank_mask:0x5\n"
    "v_smfmac_f32_16x16x64_bf16 v[36:39], v[28:31], a[136:143], v19\n"
    "v_mov_b32_dpp v20, v24 row_newbcast:9 row_mask:0xf bank_mask:0xa\n"
    "v_mov_b32_dpp v21, v24 row_newbcast:11 row_mask:0xf bank_mask:0xa\n"
    "v_smfmac_f32_16x16x64_bf16 v[52:55], v[28:31], a[168:175], v19\n"
    "v_mov_b32_dpp v22, v24 row_newbcast:13 row_mask:0xf bank_mask:0xa\n"
    "v_mov_b32_dpp v23, v24 row_newbcast:15 row_mask:0xf bank_mask:0xa\n"
    "v_smfmac_f32_16x16x64_bf16 v[40:43], v[28:31], a[144:151], v19\n"
    "v_smfmac_f32_16x16x64_bf16 v[56:59], v[28:31], a[176:183], v19\n"
    "v_smfmac_f32_16x16x64_bf16 v[44:47], v[28:31], a[152:159], v19\n"
    "v_smfmac_f32_16x16x64_bf16 v[60:63], v[28:31], a[184:191], v19\n"
    "v_smfmac_f32_16x16x64_bf16 v[32:35], v[20:23], a[192:199], v19\n"
    "v_smfmac_f32_16x16x64_bf16 v[48:51], v[20:23], a[224:231], v19\n"
    "v_smfmac_f32_16x16x64_bf16 v[36:39], v[20:23], a[200:207], v19\n"
    "v_smfmac_f32_16x16x64_bf16 v[52:55], v[20:23], a[232:239], v19\n"
    "v_smfmac_f32_16x16x64_bf16 v[40:43], v[20:23], a[208:215], v19\n"
    "v_smfmac_f32_16x16x64_bf16 v[56:59], v[20:23], a[240:247], v19\n"
    "v_smfmac_f32_16x16x64_bf16 v[44:47], v[20:23], a[216:223], v19\n"
    "v_smfmac_f32_16x16x64_bf16 v[60:63], v[20:23], a[248:255], v19\n"
    "s_nop 1\n"
    "v_permlane16_swap_b32 v32, v48\n"
    "v_add_f32 v28, v32, v48\n"
    "v_permlane16_swap_b32 v36, v52\n"
    "v_add_f32 v29, v36, v52\n"
    "v_permlane16_swap_b32 v40, v56\n"
    "v_add_f32 v30, v40, v56\n"
    "v_permlane16_swap_b32 v44, v60\n"
    "v_add_f32 v31, v44, v60\n"
    "s_mov_b32 exec_hi, 0\n"
    "ds_write_b128 v12, v[28:31] offset:4096\n"
    "s_mov_b32 exec_hi, -1\n"
    "s_cmp_lg_u32 s51, 0\n"
    "s_waitcnt lgkmcnt(0)\n"
    "s_barrier\n"
    "s_cbranch_scc1 Cqx_%=\n"
    "ds_read2st64_b64 v[32:35], v13 offset0:8 offset1:9\n"
    "ds_read2st64_b64 v[36:39], v13 offset0:10 offset1:11\n"
    "s_waitcnt lgkmcnt(1)\n"
    "v_pk_add_f32 v[32:33], v[32:33], v[34:35]\n"
    "s_waitcnt lgkmcnt(0)\n"
    "v_pk_add_f32 v[36:37], v[36:37], v[38:39]\n"
    "v_pk_add_f32 v[32:33], v[32:33], v[36:37]\n"
    "v_cvt_pk_bf16_f32 v29, v32, v33\n"
    "v_min_u32 v28, -2, v29\n"
    "v_min_u32 v29, -2, v29\n"
    "s_nop 1\n"
    "v_permlane16_swap_b32 v28, v29\n"
    "v_mov_b32 v30, v28\n"
    "v_mov_b32 v31, v29\n"
    "s_nop 0\n"
    "v_permlane32_swap_b32 v28, v30\n"
    "v_permlane32_swap_b32 v29, v31\n"
    "s_mov_b64 exec, 0xffff\n"
    "buffer_store_dwordx4 v[28:31], v0, s[40:43], s73 offen sc1\n"
    "s_mov_b64 exec, -1\n"
    "s_nop 0\n"
    "Cqx_%=:\n"
    "v_mov_b32 v18, v15\n"
    "s_cmp_lg_u32 s54, 0\n"
    "s_nop 0\n"
    "s_cbranch_scc1 CF2_%=\n"
    "Cnext_%=:\n"
    "s_add_u32 s54, s54, 1\n"
    "s_mov_b32 s53, s54\n"
    "s_cmp_lt_u32 s54, 8\n"
    "s_cbranch_scc1 Ctop_%=\n"
    "v_mov_b32 v18, v16\n"
    "s_mov_b32 s53, 15\n"
    "s_cmp_eq_u32 s54, 8\n"
    "s_cbranch_scc1 Ctop_%=\n"
    "s_waitcnt vmcnt(0) lgkmcnt(0)\n"
    "Lfused_code_end:\n"
    :: "s"(a.x), "s"(a.wq), "s"(a.wo), "s"(a.wg), "s"(a.wu), "s"(a.wd), "s"(a.out), "s"(a.ws), "s"(j), "s"(w), "s"(f)
    : ALL_REGS);
  } else {
    const int cu = blockIdx.x - NCHAIN;
    const int head = cu & 31, split = cu >> 5, wv = split * 4 + w;
    const u32 key0 = (SK * wv) / (4 * NSPLIT), key1 = (SK * (wv + 1)) / (4 * NSPLIT);
    const u16* kb = a.k + ((size_t)head * SK + key0) * DH;
    const u16* vb = a.v + ((size_t)head * SK + key0) * DH;
    const u32 nbytes = (key1 - key0) * (DH * 2);
    const u32 ring = 8704 + w * 37888, qoff = WS_Q + head * 256, rec = WS_O + (head * NSPLIT + split) * OREC, mw = w * 512;
    const u32 st = (9u << 20) + (cu * 4 + w) * 1024;
    asm volatile(
    "s_memrealtime s[98:99]\n"
    "s_mov_b64 s[44:45], %1\n"
    "s_mov_b32 s46, %3\n"
    "s_mov_b32 s47, 0x00020000\n"
    "v_mbcnt_lo_u32_b32 v3, -1, 0\n"
    "v_mbcnt_hi_u32_b32 v3, -1, v3\n"
    "s_mov_b32 s52, 4096\n"
    "s_mov_b32 s53, 8192\n"
    "s_mov_b32 s54, 12288\n"
    "s_mov_b32 s55, 16384\n"
    "s_mov_b32 s56, 20480\n"
    "s_mov_b32 s57, 24576\n"
    "s_mov_b32 s58, 28672\n"
    "s_mov_b32 s59, 32768\n"
    "s_mov_b32 s60, 36864\n"
    "v_lshlrev_b32 v1, 4, v3\n"
    "v_and_b32 v2, 0xf0, v1\n"
    "v_bfe_u32 v8, v3, 4, 1\n"
    "v_lshl_add_u32 v2, v8, 10, v2\n"
    "v_lshrrev_b32 v8, 5, v3\n"
    "v_lshl_add_u32 v2, v8, 8, v2\n"
    "s_mov_b32 s64, %4\n"
    "s_mov_b32 s62, %4\n"
    "s_add_u32 s63, %4, 7168\n"
    "s_mov_b64 s[48:49], %2\n"
    "s_mov_b32 s50, 1048576\n"
    "s_mov_b32 s51, 0x00020000\n"
    "s_mov_b32 s65, %5\n"
    "s_mov_b32 s61, 0\n"
    "s_mov_b32 s96, 259\n"
    "s_lshl_b32 s82, %8, 2\n"
    "s_waitcnt lgkmcnt(0)\n"
    "s_lshl_b32 s95, s98, 4\n"
    "s_sub_u32 s95, s95, 1813\n"
    "Ltop_%=:\n"
    "s_mul_i32 s97, s96, 0\n"
    "s_add_u32 s97, s97, s95\n"
    "Lg1_%=:\n"
    "s_memrealtime s[98:99]\n"
    "s_waitcnt lgkmcnt(0)\n"
    "s_lshl_b32 s98, s98, 4\n"
    "s_sub_u32 s98, s98, s97\n"
    "s_cmp_lt_i32 s98, 0\n"
    "s_cbranch_scc1 Lg1_%=\n"
    "s_add_u32 m0, s62, 0\n"
    "s_nop 0\n"
    "buffer_load_dwordx4 v1, s[44:47], s60 offen offset:0 nt lds\n"
    "s_waitcnt vmcnt(7)\n"
    "v_smfmac_f32_16x16x64_bf16 v[28:31], v[12:15], v[220:227], v34\n"
    "v_and_b32_dpp v8, v37, v32 row_newbcast:14 row_mask:0xf bank_mask:0xf\n"
    "v_and_b32_dpp v9, v37, v33 row_newbcast:14 row_mask:0xf bank_mask:0xf\n"
    "v_and_b32_dpp v10, v37, v32 row_newbcast:15 row_mask:0xf bank_mask:0xf\n"
    "v_and_b32_dpp v11, v37, v33 row_newbcast:15 row_mask:0xf bank_mask:0xf\n"
    "s_mul_i32 s97, s96, 1\n"
    "s_add_u32 s97, s97, s95\n"
    "Lg2_%=:\n"
    "s_memrealtime s[98:99]\n"
    "s_waitcnt lgkmcnt(0)\n"
    "s_lshl_b32 s98, s98, 4\n"
    "s_sub_u32 s98, s98, s97\n"
    "s_cmp_lt_i32 s98, 0\n"
    "s_cbranch_scc1 Lg2_%=\n"
    "s_add_u32 m0, s62, 1024\n"
    "s_nop 0\n"
    "buffer_load_dwordx4 v2, s[44:47], 0 offen offset:0 nt lds\n"
    "s_add_u32 s68, s61, 6\n"
    "s_lshl_b32 s68, s68, 13\n"
    "s_add_u32 s68, s68, s65\n"
    "v_lshrrev_b32 v3, 6, v1\n"
    "v_lshlrev_b32 v3, 4, v3\n"
    "buffer_load_dwordx4 v[4:7], v3, s[48:51], s68 offen sc1\n"
    "s_waitcnt vmcnt(7)\n"
    "v_smfmac_f32_16x16x64_bf16 v[28:31], v[8:11], v[228:235], v34\n"
    "v_and_b32_dpp v12, v38, v32 row_newbcast:0 row_mask:0xf bank_mask:0xf\n"
    "v_and_b32_dpp v13, v38, v33 row_newbcast:0 row_mask:0xf bank_mask:0xf\n"
    "v_and_b32_dpp v14, v38, v32 row_newbcast:1 row_mask:0xf bank_mask:0xf\n"
    "v_and_b32_dpp v15, v38, v33 row_newbcast:1 row_mask:0xf bank_mask:0xf\n"
    "s_mul_i32 s97, s96, 2\n"
    "s_add_u32 s97, s97, s95\n"
    "Lg3_%=:\n"
    "s_memrealtime s[98:99]\n"
    "s_waitcnt lgkmcnt(0)\n"
    "s_lshl_b32 s98, s98, 4\n"
    "s_sub_u32 s98, s98, s97\n"
    "s_cmp_lt_i32 s98, 0\n"
    "s_cbranch_scc1 Lg3_%=\n"
    "s_add_u32 m0, s62, 1536\n"
    "s_nop 0\n"
    "buffer_load_dwordx4 v2, s[44:47], 0 offen offset:512 nt lds\n"
    "s_mul_i32 s97, s96, 3\n"
    "s_add_u32 s97, s97, s95\n"
    "Lg4_%=:\n"
    "s_memrealtime s[98:99]\n"
    "s_waitcnt lgkmcnt(0)\n"
    "s_lshl_b32 s98, s98, 4\n"
    "s_sub_u32 s98, s98, s97\n"
    "s_cmp_lt_i32 s98, 0\n"
    "s_cbranch_scc1 Lg4_%=\n"
    "s_add_u32 m0, s62, 1024\n"
    "s_nop 0\n"
    "buffer_load_dwordx4 v2, s[44:47], 0 offen offset:2048 nt lds\n"
    "s_waitcnt vmcnt(7)\n"
    "v_smfmac_f32_16x16x64_bf16 v[28:31], v[12:15], v[236:243], v34\n"
    "v_and_b32_dpp v8, v38, v32 row_newbcast:2 row_mask:0xf bank_mask:0xf\n"
    "v_and_b32_dpp v9, v38, v33 row_newbcast:2 row_mask:0xf bank_mask:0xf\n"
    "v_and_b32_dpp v10, v38, v32 row_newbcast:3 row_mask:0xf bank_mask:0xf\n"
    "v_and_b32_dpp v11, v38, v33 row_newbcast:3 row_mask:0xf bank_mask:0xf\n"
    "s_mul_i32 s97, s96, 4\n"
    "s_add_u32 s97, s97, s95\n"
    "Lg5_%=:\n"
    "s_memrealtime s[98:99]\n"
    "s_waitcnt lgkmcnt(0)\n"
    "s_lshl_b32 s98, s98, 4\n"
    "s_sub_u32 s98, s98, s97\n"
    "s_cmp_lt_i32 s98, 0\n"
    "s_cbranch_scc1 Lg5_%=\n"
    "s_add_u32 m0, s62, 1536\n"
    "s_nop 0\n"
    "buffer_load_dwordx4 v2, s[44:47], 0 offen offset:2560 nt lds\n"
    "s_mul_i32 s97, s96, 5\n"
    "s_add_u32 s97, s97, s95\n"
    "Lg6_%=:\n"
    "s_memrealtime s[98:99]\n"
    "s_waitcnt lgkmcnt(0)\n"
    "s_lshl_b32 s98, s98, 4\n"
    "s_sub_u32 s98, s98, s97\n"
    "s_cmp_lt_i32 s98, 0\n"
    "s_cbranch_scc1 Lg6_%=\n"
    "s_add_u32 m0, s62, 5120\n"
    "s_nop 0\n"
    "buffer_load_dwordx4 v2, s[44:47], s52 offen offset:0 nt lds\n"
    "s_waitcnt vmcnt(7)\n"
    "v_smfmac_f32_16x16x64_bf16 v[28:31], v[8:11], v[244:251], v34\n"
    "s_add_u32 m0, s62, 5632\n"
    "s_nop 0\n"
    "buffer_load_dwordx4 v2, s[44:47], s52 offen offset:512 nt lds\n"
    "s_cmp_eq_u32 s61, 0\n"
    "s_nop 3\n"
    "s_cbranch_scc1 Lsetup_%=\n"
    "v_mov_b32 v8, 0\n"
    "v_mov_b32 v9, 1\n"
    "v_mov_b32 v3, 0\n"
    "v_and_b32 v36, 0x1f0, v39\n"
    "s_add_u32 s69, s61, 0\n"
    "s_mul_i32 s69, s69, 0x9e3779b1\n"
    "s_add_u32 s69, s69, s92\n"
    "s_mov_b32 exec_hi, 0\n"
    "ds_write_b128 v39, v[28:31] offset:2112\n"
    "s_mov_b64 exec, 1\n"
    "ds_add_rtn_u32 v9, v8, v9 offset:4192\n"
    "s_mov_b64 exec, -1\n"
    "ds_read_b128 v[12:15], v3 offset:4160\n"
    "ds_read_b128 v[16:19], v36 offset:2112\n"
    "ds_read_b128 v[20:23], v36 offset:2624\n"
    "ds_read_b128 v[24:27], v36 offset:3136\n"
    "s_waitcnt lgkmcnt(4)\n"
    "v_readfirstlane_b32 s68, v9\n"
    "ds_read_b128 v[8:11], v3 offset:4176\n"
    "ds_read_b128 v[28:31], v36 offset:3648\n"
    "s_and_b32 s68, s68, 3\n"
    "s_cmp_eq_u32 s68, 3\n"
    "s_cbranch_scc0 Lf1w_%=\n"
    "s_add_u32 s68, s66, 86016\n"
    "s_waitcnt lgkmcnt(5)\n"
    "v_max3_f32 v37, v12, v13, v14\n"
    "v_max_f32 v37, v37, v15\n"
    "v_sub_f32 v12, v12, v37\n"
    "v_sub_f32 v13, v13, v37\n"
    "v_sub_f32 v14, v14, v37\n"
    "v_sub_f32 v15, v15, v37\n"
    "v_exp_f32 v12, v12\n"
    "v_exp_f32 v13, v13\n"
    "v_exp_f32 v14, v14\n"
    "v_exp_f32 v15, v15\n"
    "s_waitcnt lgkmcnt(1)\n"
    "v_mul_f32 v38, v12, v8\n"
    "v_fmac_f32 v38, v13, v9\n"
    "v_fmac_f32 v38, v14, v10\n"
    "v_fmac_f32 v38, v15, v11\n"
    "v_rcp_f32 v8, v38\n"
    "v_log_f32 v38, v38\n"
    "v_pk_mul_f32 v[16:17], v[12:13], v[16:17] op_sel_hi:[0,1]\n"
    "v_pk_mul_f32 v[18:19], v[12:13], v[18:19] op_sel_hi:[0,1]\n"
    "s_waitcnt lgkmcnt(0)\n"
    "v_pk_fma_f32 v[16:17], v[12:13], v[20:21], v[16:17] op_sel:[1,0,0] op_sel_hi:[1,1,1]\n"
    "v_pk_fma_f32 v[18:19], v[12:13], v[22:23], v[18:19] op_sel:[1,0,0] op_sel_hi:[1,1,1]\n"
    "v_pk_fma_f32 v[16:17], v[14:15], v[24:25], v[16:17] op_sel_hi:[0,1,1]\n"
    "v_pk_fma_f32 v[18:19], v[14:15], v[26:27], v[18:19] op_sel_hi:[0,1,1]\n"
    "v_pk_fma_f32 v[16:17], v[14:15], v[28:29], v[16:17] op_sel:[1,0,0] op_sel_hi:[1,1,1]\n"
    "v_pk_fma_f32 v[18:19], v[14:15], v[30:31], v[18:19] op_sel:[1,0,0] op_sel_hi:[1,1,1]\n"
    "v_pk_mul_f32 v[16:17], v[8:9], v[16:17] op_sel_hi:[0,1]\n"
    "v_pk_mul_f32 v[18:19], v[8:9], v[18:19] op_sel_hi:[0,1]\n"
    "v_cvt_pk_bf16_f32 v20, v16, v17\n"
    "v_cvt_pk_bf16_f32 v22, v16, v17\n"
    "v_cvt_pk_bf16_f32 v21, v18, v19\n"
    "v_cvt_pk_bf16_f32 v23, v18, v19\n"
    "v_add_f32 v38, v38, v37\n"
    "v_permlane16_swap_b32 v20, v22\n"
    "v_permlane16_swap_b32 v21, v23\n"
    "v_add3_u32 v24, v20, v22, v38\n"
    "v_add3_u32 v25, v21, v23, s69\n"
    "v_add_u32 v24, v24, v25\n"
    "v_cndmask_b32 v20, v20, v38, s[80:81]\n"
    "v_cndmask_b32 v22, v22, v38, s[80:81]\n"
    "v_mov_b32_dpp v23, v24 row_ror:8 row_mask:0xe bank_mask:0xf\n"
    "s_mov_b64 exec, 0xffffff\n"
    "v_cndmask_b32 v21, v21, v24, s[80:81]\n"
    "buffer_store_dwordx4 v[20:23], v1, s[48:51], s68 offen sc1\n"
    "s_mov_b64 exec, -1\n"
    "s_nop 0\n"
    "Lf1w_%=:\n"
    "s_waitcnt lgkmcnt(0)\n"
    "Lf1x_%=:\n"
    "s_waitcnt vmcnt(5)\n"
    "Lqac_%=:\n"
    "v_max3_u32 v3, v4, v5, v6\n"
    "v_max_u32 v3, v3, v7\n"
    "v_cmp_eq_u32 vcc, -1, v3\n"
    "s_cbranch_vccz Lqad_%=\n"
    "s_nop 3\n"
    "Lqar_%=:\n"
    "s_sub_u32 s67, s67, 1\n"
    "s_cmp_lt_i32 s67, 0\n"
    "s_cbranch_scc1 Lqad_%=\n"
    "s_add_u32 s68, s61, 6\n"
    "s_lshl_b32 s68, s68, 13\n"
    "s_add_u32 s68, s68, s65\n"
    "v_lshrrev_b32 v3, 6, v1\n"
    "v_lshlrev_b32 v3, 4, v3\n"
    "buffer_load_dwordx4 v[4:7], v3, s[48:51], s68 offen sc1\n"
    "s_memrealtime s[98:99]\n"
    "s_mul_i32 s97, s96, 7\n"
    "s_waitcnt lgkmcnt(0)\n"
    "s_lshl_b32 s98, s98, 4\n"
    "s_sub_u32 s98, s98, s97\n"
    "s_sub_u32 s97, s98, s95\n"
    "s_cmp_gt_i32 s97, 0\n"
    "s_cselect_b32 s95, s98, s95\n"
    "s_waitcnt vmcnt(0)\n"
    "s_branch Lqac_%=\n"
    "Lqad_%=:\n"
    "v_cndmask_b32 v8, 0, v4, s[70:71]\n"
    "v_cndmask_b32 v9, 0, v5, s[70:71]\n"
    "v_cndmask_b32 v10, 0, v6, s[70:71]\n"
    "v_cndmask_b32 v11, 0, v7, s[70:71]\n"
    "v_cndmask_b32 v12, 0, v4, s[72:73]\n"
    "v_cndmask_b32 v13, 0, v5, s[72:73]\n"
    "v_cndmask_b32 v14, 0, v6, s[72:73]\n"
    "v_cndmask_b32 v15, 0, v7, s[72:73]\n"
    "v_cndmask_b32 v16, 0, v4, s[74:75]\n"
    "v_cndmask_b32 v17, 0, v5, s[74:75]\n"
    "v_cndmask_b32 v18, 0, v6, s[74:75]\n"
    "v_cndmask_b32 v19, 0, v7, s[74:75]\n"
    "v_cndmask_b32 v4, 0, v4, s[76:77]\n"
    "v_cndmask_b32 v5, 0, v5, s[76:77]\n"
    "v_cndmask_b32 v6, 0, v6, s[76:77]\n"
    "v_cndmask_b32 v7, 0, v7, s[76:77]\n"
    "v_add_u32 v3, s64, v1\n"
    "v_add_u32 v38, s63, v1\n"
    "v_mfma_f32_16x16x32_bf16 v[20:23], a[0:3], v[8:11], 0\n"
    "ds_read_b128 a[0:3], v3 offset:14336\n"
    "v_mfma_f32_16x16x32_bf16 v[20:23], a[4:7], v[12:15], v[20:23]\n"
    "ds_read_b128 a[4:7], v3 offset:15360\n"
    "v_mfma_f32_16x16x32_bf16 v[20:23], a[8:11], v[16:19], v[20:23]\n"
    "ds_read_b128 a[8:11], v3 offset:16384\n"
    "v_mfma_f32_16x16x32_bf16 v[20:23], a[12:15], v[4:7], v[20:23]\n"
    "ds_read_b128 a[12:15], v3 offset:17408\n"
    "v_mfma_f32_16x16x32_bf16 v[24:27], a[16:19], v[8:11], 0\n"
    "ds_read_b128 a[16:19], v3 offset:18432\n"
    "s_waitcnt lgkmcnt(4)\n"
    "s_add_u32 m0, s64, 12288\n"
    "s_nop 0\n"
    "buffer_load_dwordx4 v2, s[44:47], s52 offen offset:2048 nt lds\n"
    "v_mfma_f32_16x16x32_bf16 v[24:27], a[20:23], v[12:15], v[24:27]\n"
    "ds_read_b128 a[20:23], v3 offset:19456\n"
    "v_mfma_f32_16x16x32_bf16 v[24:27], a[24:27], v[16:19], v[24:27]\n"
    "ds_read_b128 a[24:27], v3 offset:20480\n"
    "v_mfma_f32_16x16x32_bf16 v[24:27], a[28:31], v[4:7], v[24:27]\n"
    "ds_read_b128 a[28:31], v3 offset:21504\n"
    "s_waitcnt lgkmcnt(6)\n"
    "s_add_u32 m0, s64, 12800\n"
    "s_nop 0\n"
    "buffer_load_dwordx4 v2, s[44:47], s52 offen offset:2560 nt lds\n"
    "v_mfma_f32_16x16x32_bf16 v[28:31], a[32:35], v[8:11], 0\n"
    "ds_read_b128 a[32:35], v38 offset:0\n"
    "v_add_f32_dpp v36, v21, v20 row_ror:12 row_mask:0xf bank_mask:0x1\n"
    "v_mfma_f32_16x16x32_bf16 v[28:31], a[36:39], v[12:15], v[28:31]\n"
    "ds_read_b128 a[36:39], v38 offset:1024\n"
    "v_add_f32_dpp v36, v22, v36 row_ror:8 row_mask:0xf bank_mask:0x1\n"
    "v_mfma_f32_16x16x32_bf16 v[28:31], a[40:43], v[16:19], v[28:31]\n"
    "ds_read_b128 a[40:43], v38 offset:2048\n"
    "s_waitcnt lgkmcnt(8)\n"
    "s_add_u32 m0, s64, 16384\n"
    "s_nop 0\n"
    "buffer_load_dwordx4 v2, s[44:47], s53 offen offset:0 nt lds\n"
    "v_add_f32_dpp v36, v23, v36 row_ror:4 row_mask:0xf bank_mask:0x1\n"
    "v_mfma_f32_16x16x32_bf16 v[28:31], a[44:47], v[4:7], v[28:31]\n"
    "ds_read_b128 a[44:47], v38 offset:3072\n"
    "v_mfma_f32_16x16x32_bf16 v[20:23], a[48:51], v[8:11], 0\n"
    "ds_read_b128 a[48:51], v38 offset:4096\n"
    "v_add_f32_dpp v36, v26, v25 row_ror:12 row_mask:0xf bank_mask:0x2\n"
    "v_mfma_f32_16x16x32_bf16 v[20:23], a[52:55], v[12:15], v[20:23]\n"
    "ds_read_b128 a[52:55], v38 offset:5120\n"
    "v_add_f32_dpp v36, v27, v36 row_ror:8 row_mask:0xf bank_mask:0x2\n"
    "v_mfma_f32_16x16x32_bf16 v[20:23], a[56:59], v[16:19], v[20:23]\n"
    "ds_read_b128 a[56:59], v38 offset:6144\n"
    "v_add_f32_dpp v36, v24, v36 row_ror:4 row_mask:0xf bank_mask:0x2\n"
    "v_mfma_f32_16x16x32_bf16 v[20:23], a[60:63], v[4:7], v[20:23]\n"
    "v_mfma_f32_16x16x32_bf16 v[24:27], a[64:67], v[8:11], 0\n"
    "s_waitcnt lgkmcnt(11)\n"
    "s_add_u32 m0, s64, 16896\n"
    "s_nop 0\n"
    "buffer_load_dwordx4 v2, s[44:47], s53 offen offset:512 nt lds\n"
    "v_add_f32_dpp v36, v31, v30 row_ror:12 row_mask:0xf bank_mask:0x4\n"
    "v_mfma_f32_16x16x32_bf16 v[24:27], a[68:71], v[12:15], v[24:27]\n"
    "s_nop 0\n"
    "v_add_f32_dpp v36, v28, v36 row_ror:8 row_mask:0xf bank_mask:0x4\n"
    "v_mfma_f32_16x16x32_bf16 v[24:27], a[72:75], v[16:19], v[24:27]\n"
    "s_nop 0\n"
    "v_add_f32_dpp v36, v29, v36 row_ror:4 row_mask:0xf bank_mask:0x4\n"
    "v_mfma_f32_16x16x32_bf16 v[24:27], a[76:79], v[4:7], v[24:27]\n"
    "s_waitcnt lgkmcnt(0)\n"
    "v_mfma_f32_16x16x32_bf16 v[28:31], a[80:83], v[8:11], 0\n"
    "v_add_f32_dpp v36, v20, v23 row_ror:12 row_mask:0xf bank_mask:0x8\n"
    "v_mfma_f32_16x16x32_bf16 v[28:31], a[84:87], v[12:15], v[28:31]\n"
    "s_nop 0\n"
    "v_add_f32_dpp v36, v21, v36 row_ror:8 row_mask:0xf bank_mask:0x8\n"
    "v_mfma_f32_16x16x32_bf16 v[28:31], a[88:91], v[16:19], v[28:31]\n"
    "s_nop 0\n"
    "v_add_f32_dpp v36, v22, v36 row_ror:4 row_mask:0xf bank_mask:0x8\n"
    "v_mfma_f32_16x16x32_bf16 v[28:31], a[92:95], v[4:7], v[28:31]\n"
    "v_mfma_f32_16x16x32_bf16 v[20:23], a[96:99], v[8:11], 0\n"
    "s_add_u32 m0, s64, 16384\n"
    "s_nop 0\n"
    "buffer_load_dwordx4 v2, s[44:47], s53 offen offset:2048 nt lds\n"
    "v_add_f32_dpp v37, v25, v24 row_ror:12 row_mask:0xf bank_mask:0x1\n"
    "v_mfma_f32_16x16x32_bf16 v[20:23], a[100:103], v[12:15], v[20:23]\n"
    "s_nop 0\n"
    "v_add_f32_dpp v37, v26, v37 row_ror:8 row_mask:0xf bank_mask:0x1\n"
    "v_mfma_f32_16x16x32_bf16 v[20:23], a[104:107], v[16:19], v[20:23]\n"
    "s_nop 0\n"
    "v_add_f32_dpp v37, v27, v37 row_ror:4 row_mask:0xf bank_mask:0x1\n"
    "v_mfma_f32_16x16x32_bf16 v[20:23], a[108:111], v[4:7], v[20:23]\n"
    "v_mfma_f32_16x16x32_bf16 v[24:27], a[112:115], v[8:11], 0\n"
    "v_add_f32_dpp v37, v30, v29 row_ror:12 row_mask:0xf bank_mask:0x2\n"
    "v_mfma_f32_16x16x32_bf16 v[24:27], a[116:119], v[12:15], v[24:27]\n"
    "s_nop 0\n"
    "v_add_f32_dpp v37, v31, v37 row_ror:8 row_mask:0xf bank_mask:0x2\n"
    "v_mfma_f32_16x16x32_bf16 v[24:27], a[120:123], v[16:19], v[24:27]\n"
    "s_nop 0\n"
    "v_add_f32_dpp v37, v28, v37 row_ror:4 row_mask:0xf bank_mask:0x2\n"
    "v_mfma_f32_16x16x32_bf16 v[24:27], a[124:127], v[4:7], v[24:27]\n"
    "v_mfma_f32_16x16x32_bf16 v[28:31], a[128:131], v[8:11], 0\n"
    "s_add_u32 m0, s64, 16896\n"
    "s_nop 0\n"
    "buffer_load_dwordx4 v2, s[44:47], s53 offen offset:2560 nt lds\n"
    "v_add_f32_dpp v37, v23, v22 row_ror:12 row_mask:0xf bank_mask:0x4\n"
    "v_mfma_f32_16x16x32_bf16 v[28:31], a[132:135], v[12:15], v[28:31]\n"
    "s_nop 0\n"
    "v_add_f32_dpp v37, v20, v37 row_ror:8 row_mask:0xf bank_mask:0x4\n"
    "v_mfma_f32_16x16x32_bf16 v[28:31], a[136:139], v[16:19], v[28:31]\n"
    "s_nop 0\n"
    "v_add_f32_dpp v37, v21, v37 row_ror:4 row_mask:0xf bank_mask:0x4\n"
    "v_mfma_f32_16x16x32_bf16 v[28:31], a[140:143], v[4:7], v[28:31]\n"
    "v_mfma_f32_16x16x32_bf16 v[20:23], a[144:147], v[8:11], 0\n"
    "v_add_f32_dpp v37, v24, v27 row_ror:12 row_mask:0xf bank_mask:0x8\n"
    "s_nop 1\n"
    "v_add_f32_dpp v37, v25, v37 row_ror:8 row_mask:0xf bank_mask:0x8\n"
    "s_nop 1\n"
    "v_add_f32_dpp v37, v26, v37 row_ror:4 row_mask:0xf bank_mask:0x8\n"
    "v_add_f32_dpp v38, v29, v28 row_ror:12 row_mask:0xf bank_mask:0x1\n"
    "s_nop 1\n"
    "v_add_f32_dpp v38, v30, v38 row_ror:8 row_mask:0xf bank_mask:0x1\n"
    "s_nop 1\n"
    "v_add_f32_dpp v38, v31, v38 row_ror:4 row_mask:0xf bank_mask:0x1\n"
    "s_nop 1\n"
    "v_add_f32_dpp v38, v22, v21 row_ror:12 row_mask:0xf bank_mask:0x2\n"
    "s_nop 1\n"
    "v_add_f32_dpp v38, v23, v38 row_ror:8 row_mask:0xf bank_mask:0x2\n"
    "s_nop 1\n"
    "v_add_f32_dpp v38, v20, v38 row_ror:4 row_mask:0xf bank_mask:0x2\n"
    "v_cvt_pk_bf16_f32 v8, v36, v37\n"
    "v_cvt_pk_bf16_f32 v9, v38, v38\n"
    "v_lshlrev_b32 v36, 16, v8\n"
    "v_and_b32 v37, s87, v8\n"
    "v_and_b32 v38, s87, v9\n"
    "v_mul_f32 v36, s84, v36\n"
    "v_mul_f32 v37, s84, v37\n"
    "v_mul_f32 v38, s84, v38\n"
    "v_cvt_pk_bf16_f32 v8, v36, v37\n"
    "v_cvt_pk_bf16_f32 v9, v38, v38\n"
    "v_mov_b32 v10, s86\n"
    "v_lshlrev_b32 v36, 16, v8\n"
    "v_and_b32 v37, s87, v8\n"
    "v_and_b32 v38, s87, v9\n"
    "v_cndmask_b32 v38, v10, v38, s[78:79]\n"
    "v_max3_f32 v8, v36, v37, v38\n"
    "s_add_u32 m0, s64, 20480\n"
    "s_nop 0\n"
    "buffer_load_dwordx4 v2, s[44:47], s54 offen offset:0 nt lds\n"
    "v_max_f32_dpp v8, v8, v8 row_ror:8 row_mask:0xf bank_mask:0xf\n"
    "s_nop 1\n"
    "v_max_f32_dpp v8, v8, v8 row_ror:4 row_mask:0xf bank_mask:0xf\n"
    "s_nop 1\n"
    "v_max_f32_dpp v8, v8, v8 row_ror:2 row_mask:0xf bank_mask:0xf\n"
    "s_nop 1\n"
    "v_max_f32_dpp v9, v8, v8 row_ror:1 row_mask:0xf bank_mask:0xf\n"
    "v_max_f32_dpp v8, v8, v8 row_ror:1 row_mask:0xf bank_mask:0xf\n"
    "s_nop 1\n"
    "v_permlane16_swap_b32 v8, v9\n"
    "v_max_f32 v10, v8, v9\n"
    "v_max_f32 v8, v8, v9\n"
    "s_nop 1\n"
    "v_permlane32_swap_b32 v8, v10\n"
    "v_max_f32 v8, v8, v10\n"
    "v_mul_f32 v16, s85, v8\n"
    "v_fma_f32 v38, v38, s85, -v16\n"
    "v_fma_f32 v36, v36, s85, -v16\n"
    "v_fma_f32 v37, v37, s85, -v16\n"
    "v_exp_f32 v38, v38\n"
    "v_exp_f32 v36, v36\n"
    "v_exp_f32 v37, v37\n"
    "v_add_f32 v17, v38, v36\n"
    "v_cvt_pk_bf16_f32 v38, v38, v38\n"
    "v_add_f32 v17, v17, v37\n"
    "v_cvt_pk_bf16_f32 v36, v36, v36\n"
    "v_cvt_pk_bf16_f32 v37, v37, v37\n"
    "v_and_b32_dpp v19, v38, v35 row_newbcast:4 row_mask:0xf bank_mask:0xf\n"
    "s_mov_b32 vcc_lo, 0x3030303\n"
    "s_mov_b32 vcc_hi, 0x3030303\n"
    "v_cndmask_b32 v8, 0, v19, vcc\n"
    "s_mov_b32 vcc_lo, 0xc0c0c0c\n"
    "s_mov_b32 vcc_hi, 0xc0c0c0c\n"
    "v_cndmask_b32 v9, 0, v19, vcc\n"
    "s_mov_b32 vcc_lo, 0x30303030\n"
    "s_mov_b32 vcc_hi, 0x30303030\n"
    "v_cndmask_b32 v10, 0, v19, vcc\n"
    "s_mov_b32 vcc_lo, 0xc0c0c0c0\n"
    "s_mov_b32 vcc_hi, 0xc0c0c0c0\n"
    "v_cndmask_b32 v11, 0, v19, vcc\n"
    "v_and_b32_dpp v12, v36, v32 row_newbcast:14 row_mask:0xf bank_mask:0xf\n"
    "v_and_b32_dpp v13, v36, v33 row_newbcast:14 row_mask:0xf bank_mask:0xf\n"
    "v_and_b32_dpp v14, v36, v32 row_newbcast:15 row_mask:0xf bank_mask:0xf\n"
    "v_and_b32_dpp v15, v36, v33 row_newbcast:15 row_mask:0xf bank_mask:0xf\n"
    "v_mfma_f32_16x16x32_bf16 v[28:31], v[8:11], a[32:35], 0\n"
    "v_and_b32_dpp v8, v37, v32 row_newbcast:0 row_mask:0xf bank_mask:0xf\n"
    "v_and_b32_dpp v9, v37, v33 row_newbcast:0 row_mask:0xf bank_mask:0xf\n"
    "v_and_b32_dpp v10, v37, v32 row_newbcast:1 row_mask:0xf bank_mask:0xf\n"
    "v_and_b32_dpp v11, v37, v33 row_newbcast:1 row_mask:0xf bank_mask:0xf\n"
    "v_smfmac_f32_16x16x64_bf16 v[28:31], v[12:15], a[148:155], v34\n"
    "v_add_f32_dpp v17, v17, v17 row_ror:8 row_mask:0xf bank_mask:0xf\n"
    "v_and_b32_dpp v12, v37, v32 row_newbcast:2 row_mask:0xf bank_mask:0xf\n"
    "v_and_b32_dpp v13, v37, v33 row_newbcast:2 row_mask:0xf bank_mask:0xf\n"
    "v_and_b32_dpp v14, v37, v32 row_newbcast:3 row_mask:0xf bank_mask:0xf\n"
    "v_and_b32_dpp v15, v37, v33 row_newbcast:3 row_mask:0xf bank_mask:0xf\n"
    "v_smfmac_f32_16x16x64_bf16 v[28:31], v[8:11], a[156:163], v34\n"
    "v_add_f32_dpp v17, v17, v17 row_ror:4 row_mask:0xf bank_mask:0xf\n"
    "s_add_u32 m0, s64, 20992\n"
    "s_nop 0\n"
    "buffer_load_dwordx4 v2, s[44:47], s54 offen offset:512 nt lds\n"
    "v_and_b32_dpp v8, v37, v32 row_newbcast:4 row_mask:0xf bank_mask:0xf\n"
    "v_and_b32_dpp v9, v37, v33 row_newbcast:4 row_mask:0xf bank_mask:0xf\n"
    "v_and_b32_dpp v10, v37, v32 row_newbcast:5 row_mask:0xf bank_mask:0xf\n"
    "v_and_b32_dpp v11, v37, v33 row_newbcast:5 row_mask:0xf bank_mask:0xf\n"
    "v_smfmac_f32_16x16x64_bf16 v[28:31], v[12:15], a[164:171], v34\n"
    "v_add_f32_dpp v17, v17, v17 row_ror:2 row_mask:0xf bank_mask:0xf\n"
    "v_and_b32_dpp v12, v37, v32 row_newbcast:6 row_mask:0xf bank_mask:0xf\n"
    "v_and_b32_dpp v13, v37, v33 row_newbcast:6 row_mask:0xf bank_mask:0xf\n"
    "v_and_b32_dpp v14, v37, v32 row_newbcast:7 row_mask:0xf bank_mask:0xf\n"
    "v_and_b32_dpp v15, v37, v33 row_newbcast:7 row_mask:0xf bank_mask:0xf\n"
    "v_smfmac_f32_16x16x64_bf16 v[28:31], v[8:11], a[172:179], v34\n"
    "v_add_f32_dpp v17, v17, v17 row_ror:1 row_mask:0xf bank_mask:0xf\n"
    "v_and_b32_dpp v8, v37, v32 row_newbcast:8 row_mask:0xf bank_mask:0xf\n"
    "v_and_b32_dpp v9, v37, v33 row_newbcast:8 row_mask:0xf bank_mask:0xf\n"
    "v_and_b32_dpp v10, v37, v32 row_newbcast:9 row_mask:0xf bank_mask:0xf\n"
    "v_and_b32_dpp v11, v37, v33 row_newbcast:9 row_mask:0xf bank_mask:0xf\n"
    "v_smfmac_f32_16x16x64_bf16 v[28:31], v[12:15], a[180:187], v34\n"
    "v_mov_b32 v18, v17\n"
    "v_and_b32_dpp v12, v37, v32 row_newbcast:10 row_mask:0xf bank_mask:0xf\n"
    "v_and_b32_dpp v13, v37, v33 row_newbcast:10 row_mask:0xf bank_mask:0xf\n"
    "v_and_b32_dpp v14, v37, v32 row_newbcast:11 row_mask:0xf bank_mask:0xf\n"
    "v_and_b32_dpp v15, v37, v33 row_newbcast:11 row_mask:0xf bank_mask:0xf\n"
    "v_smfmac_f32_16x16x64_bf16 v[28:31], v[8:11], a[188:195], v34\n"
    "v_permlane16_swap_b32 v17, v18\n"
    "v_add_f32 v17, v17, v18\n"
    "v_and_b32_dpp v8, v37, v32 row_newbcast:12 row_mask:0xf bank_mask:0xf\n"
    "v_and_b32_dpp v9, v37, v33 row_newbcast:12 row_mask:0xf bank_mask:0xf\n"
    "v_and_b32_dpp v10, v37, v32 row_newbcast:13 row_mask:0xf bank_mask:0xf\n"
    "v_and_b32_dpp v11, v37, v33 row_newbcast:13 row_mask:0xf bank_mask:0xf\n"
    "v_smfmac_f32_16x16x64_bf16 v[28:31], v[12:15], a[196:203], v34\n"
    "v_mov_b32 v18, v17\n"
    "buffer_load_dwordx4 a[148:151], v2, s[44:47], s54 offen offset:2048 nt\n"
    "v_and_b32_dpp v12, v37, v32 row_newbcast:14 row_mask:0xf bank_mask:0xf\n"
    "v_and_b32_dpp v13, v37, v33 row_newbcast:14 row_mask:0xf bank_mask:0xf\n"
    "v_and_b32_dpp v14, v37, v32 row_newbcast:15 row_mask:0xf bank_mask:0xf\n"
    "v_and_b32_dpp v15, v37, v33 row_newbcast:15 row_mask:0xf bank_mask:0xf\n"
    "v_smfmac_f32_16x16x64_bf16 v[28:31], v[8:11], a[204:211], v34\n"
    "v_permlane32_swap_b32 v17, v18\n"
    "v_add_f32 v17, v17, v18\n"
    "v_and_b32_dpp v8, v38, v32 row_newbcast:0 row_mask:0xf bank_mask:0xf\n"
    "v_and_b32_dpp v9, v38, v33 row_newbcast:0 row_mask:0xf bank_mask:0xf\n"
    "v_and_b32_dpp v10, v38, v32 row_newbcast:1 row_mask:0xf bank_mask:0xf\n"
    "v_and_b32_dpp v11, v38, v33 row_newbcast:1 row_mask:0xf bank_mask:0xf\n"
    "v_smfmac_f32_16x16x64_bf16 v[28:31], v[12:15], a[212:219], v34\n"
    "v_mov_b32 v18, s82\n"
    "s_mov_b64 exec, 1\n"
    "ds_write_b32 v18, v16 offset:6272\n"
    "ds_write_b32 v18, v17 offset:6288\n"
    "s_mov_b64 exec, -1\n"
    "v_and_b32_dpp v12, v38, v32 row_newbcast:2 row_mask:0xf bank_mask:0xf\n"
    "v_and_b32_dpp v13, v38, v33 row_newbcast:2 row_mask:0xf bank_mask:0xf\n"
    "v_and_b32_dpp v14, v38, v32 row_newbcast:3 row_mask:0xf bank_mask:0xf\n"
    "v_and_b32_dpp v15, v38, v33 row_newbcast:3 row_mask:0xf bank_mask:0xf\n"
    "v_smfmac_f32_16x16x64_bf16 v[28:31], v[8:11], a[220:227], v34\n"
    "v_and_b32_dpp v8, v36, v32 row_newbcast:0 row_mask:0xf bank_mask:0xf\n"
    "v_and_b32_dpp v9, v36, v33 row_newbcast:0 row_mask:0xf bank_mask:0xf\n"
    "v_and_b32_dpp v10, v36, v32 row_newbcast:1 row_mask:0xf bank_mask:0xf\n"
    "v_and_b32_dpp v11, v36, v33 row_newbcast:1 row_mask:0xf bank_mask:0xf\n"
    "v_smfmac_f32_16x16x64_bf16 v[28:31], v[12:15], a[228:235], v34\n"
    "v_and_b32_dpp v12, v36, v32 row_newbcast:2 row_mask:0xf bank_mask:0xf\n"
    "v_and_b32_dpp v13, v36, v33 row_newbcast:2 row_mask:0xf bank_mask:0xf\n"
    "v_and_b32_dpp v14, v36, v32 row_newbcast:3 row_mask:0xf bank_mask:0xf\n"
    "v_and_b32_dpp v15, v36, v33 row_newbcast:3 row_mask:0xf bank_mask:0xf\n"
    "v_smfmac_f32_16x16x64_bf16 v[28:31], v[8:11], a[36:43], v34\n"
    "buffer_load_dwordx4 a[152:155], v2, s[44:47], s54 offen offset:2560 nt\n"
    "v_and_b32_dpp v8, v36, v32 row_newbcast:4 row_mask:0xf bank_mask:0xf\n"
    "v_and_b32_dpp v9, v36, v33 row_newbcast:4 row_mask:0xf bank_mask:0xf\n"
    "v_and_b32_dpp v10, v36, v32 row_newbcast:5 row_mask:0xf bank_mask:0xf\n"
    "v_and_b32_dpp v11, v36, v33 row_newbcast:5 row_mask:0xf bank_mask:0xf\n"
    "v_smfmac_f32_16x16x64_bf16 v[28:31], v[12:15], a[44:51], v34\n"
    "v_and_b32_dpp v12, v36, v32 row_newbcast:6 row_mask:0xf bank_mask:0xf\n"
    "v_and_b32_dpp v13, v36, v33 row_newbcast:6 row_mask:0xf bank_mask:0xf\n"
    "v_and_b32_dpp v14, v36, v32 row_newbcast:7 row_mask:0xf bank_mask:0xf\n"
    "v_and_b32_dpp v15, v36, v33 row_newbcast:7 row_mask:0xf bank_mask:0xf\n"
    "v_smfmac_f32_16x16x64_bf16 v[28:31], v[8:11], a[52:59], v34\n"
    "v_and_b32_dpp v8, v36, v32 row_newbcast:8 row_mask:0xf bank_mask:0xf\n"
    "v_and_b32_dpp v9, v36, v33 row_newbcast:8 row_mask:0xf bank_mask:0xf\n"
    "v_and_b32_dpp v10, v36, v32 row_newbcast:9 row_mask:0xf bank_mask:0xf\n"
    "v_and_b32_dpp v11, v36, v33 row_newbcast:9 row_mask:0xf bank_mask:0xf\n"
    "v_smfmac_f32_16x16x64_bf16 v[28:31], v[12:15], a[0:7], v34\n"
    "v_and_b32_dpp v12, v36, v32 row_newbcast:10 row_mask:0xf bank_mask:0xf\n"
    "v_and_b32_dpp v13, v36, v33 row_newbcast:10 row_mask:0xf bank_mask:0xf\n"
    "v_and_b32_dpp v14, v36, v32 row_newbcast:11 row_mask:0xf bank_mask:0xf\n"
    "v_and_b32_dpp v15, v36, v33 row_newbcast:11 row_mask:0xf bank_mask:0xf\n"
    "v_smfmac_f32_16x16x64_bf16 v[28:31], v[8:11], a[8:15], v34\n"
    "v_and_b32_dpp v8, v36, v32 row_newbcast:12 row_mask:0xf bank_mask:0xf\n"
    "v_and_b32_dpp v9, v36, v33 row_newbcast:12 row_mask:0xf bank_mask:0xf\n"
    "v_and_b32_dpp v10, v36, v32 row_newbcast:13 row_mask:0xf bank_mask:0xf\n"
    "v_and_b32_dpp v11, v36, v33 row_newbcast:13 row_mask:0xf bank_mask:0xf\n"
    "v_smfmac_f32_16x16x64_bf16 v[28:31], v[12:15], a[16:23], v34\n"
    "buffer_load_dwordx4 a[156:159], v2, s[44:47], s55 offen offset:0 nt\n"
    "v_smfmac_f32_16x16x64_bf16 v[28:31], v[8:11], a[24:31], v34\n"
    "s_cmp_eq_u32 s61, 0\n"
    "s_nop 6\n"
    "s_cbranch_scc1 Lf2x_%=\n"
    "v_mov_b32 v8, 0\n"
    "v_mov_b32 v9, 1\n"
    "v_mov_b32 v3, 0\n"
    "v_and_b32 v36, 0x1f0, v39\n"
    "s_add_u32 s69, s61, 7\n"
    "s_mul_i32 s69, s69, 0x9e3779b1\n"
    "s_add_u32 s69, s69, s92\n"
    "s_mov_b32 exec_hi, 0\n"
    "ds_write_b128 v39, v[28:31] offset:4224\n"
    "s_mov_b64 exec, 1\n"
    "ds_add_rtn_u32 v9, v8, v9 offset:6304\n"
    "s_mov_b64 exec, -1\n"
    "ds_read_b128 v[12:15], v3 offset:6272\n"
    "ds_read_b128 v[16:19], v36 offset:4224\n"
    "ds_read_b128 v[20:23], v36 offset:4736\n"
    "ds_read_b128 v[24:27], v36 offset:5248\n"
    "s_waitcnt lgkmcnt(4)\n"
    "v_readfirstlane_b32 s68, v9\n"
    "ds_read_b128 v[8:11], v3 offset:6288\n"
    "ds_read_b128 v[28:31], v36 offset:5760\n"
    "s_and_b32 s68, s68, 3\n"
    "s_cmp_eq_u32 s68, 3\n"
    "s_cbranch_scc0 Lf2w_%=\n"
    "s_add_u32 s68, s66, 172032\n"
    "s_waitcnt lgkmcnt(5)\n"
    "v_max3_f32 v37, v12, v13, v14\n"
    "v_max_f32 v37, v37, v15\n"
    "v_sub_f32 v12, v12, v37\n"
    "v_sub_f32 v13, v13, v37\n"
    "v_sub_f32 v14, v14, v37\n"
    "v_sub_f32 v15, v15, v37\n"
    "v_exp_f32 v12, v12\n"
    "v_exp_f32 v13, v13\n"
    "v_exp_f32 v14, v14\n"
    "v_exp_f32 v15, v15\n"
    "s_waitcnt lgkmcnt(1)\n"
    "v_mul_f32 v38, v12, v8\n"
    "v_fmac_f32 v38, v13, v9\n"
    "v_fmac_f32 v38, v14, v10\n"
    "v_fmac_f32 v38, v15, v11\n"
    "v_rcp_f32 v8, v38\n"
    "v_log_f32 v38, v38\n"
    "v_pk_mul_f32 v[16:17], v[12:13], v[16:17] op_sel_hi:[0,1]\n"
    "v_pk_mul_f32 v[18:19], v[12:13], v[18:19] op_sel_hi:[0,1]\n"
    "s_waitcnt lgkmcnt(0)\n"
    "v_pk_fma_f32 v[16:17], v[12:13], v[20:21], v[16:17] op_sel:[1,0,0] op_sel_hi:[1,1,1]\n"
    "v_pk_fma_f32 v[18:19], v[12:13], v[22:23], v[18:19] op_sel:[1,0,0] op_sel_hi:[1,1,1]\n"
    "v_pk_fma_f32 v[16:17], v[14:15], v[24:25], v[16:17] op_sel_hi:[0,1,1]\n"
    "v_pk_fma_f32 v[18:19], v[14:15], v[26:27], v[18:19] op_sel_hi:[0,1,1]\n"
    "v_pk_fma_f32 v[16:17], v[14:15], v[28:29], v[16:17] op_sel:[1,0,0] op_sel_hi:[1,1,1]\n"
    "v_pk_fma_f32 v[18:19], v[14:15], v[30:31], v[18:19] op_sel:[1,0,0] op_sel_hi:[1,1,1]\n"
    "v_pk_mul_f32 v[16:17], v[8:9], v[16:17] op_sel_hi:[0,1]\n"
    "v_pk_mul_f32 v[18:19], v[8:9], v[18:19] op_sel_hi:[0,1]\n"
    "v_cvt_pk_bf16_f32 v20, v16, v17\n"
    "v_cvt_pk_bf16_f32 v22, v16, v17\n"
    "v_cvt_pk_bf16_f32 v21, v18, v19\n"
    "v_cvt_pk_bf16_f32 v23, v18, v19\n"
    "v_add_f32 v38, v38, v37\n"
    "v_permlane16_swap_b32 v20, v22\n"
    "v_permlane16_swap_b32 v21, v23\n"
    "v_add3_u32 v24, v20, v22, v38\n"
    "v_add3_u32 v25, v21, v23, s69\n"
    "v_add_u32 v24, v24, v25\n"
    "v_cndmask_b32 v20, v20, v38, s[80:81]\n"
    "v_cndmask_b32 v22, v22, v38, s[80:81]\n"
    "v_mov_b32_dpp v23, v24 row_ror:8 row_mask:0xe bank_mask:0xf\n"
    "s_mov_b64 exec, 0xffffff\n"
    "v_cndmask_b32 v21, v21, v24, s[80:81]\n"
    "buffer_store_dwordx4 v[20:23], v1, s[48:51], s68 offen sc1\n"
    "s_mov_b64 exec, -1\n"
    "s_nop 0\n"
    "Lf2w_%=:\n"
    "s_waitcnt lgkmcnt(0)\n"
    "Lf2x_%=:\n"
    "s_cmp_ge_u32 s61, 8\n"
    "s_cbranch_scc1 Lqbr_%=\n"
    "s_mul_i32 s97, s96, 18\n"
    "s_add_u32 s97, s97, s95\n"
    "Lg7_%=:\n"
    "s_memrealtime s[98:99]\n"
    "s_waitcnt lgkmcnt(0)\n"
    "s_lshl_b32 s98, s98, 4\n"
    "s_sub_u32 s98, s98, s97\n"
    "s_cmp_lt_i32 s98, 0\n"
    "s_cbranch_scc1 Lg7_%=\n"
    "s_waitcnt vmcnt(7)\n"
    "buffer_load_dwordx4 a[160:163], v2, s[44:47], s55 offen offset:512 nt\n"
    "s_mul_i32 s97, s96, 19\n"
    "s_add_u32 s97, s97, s95\n"
    "Lg8_%=:\n"
    "s_memrealtime s[98:99]\n"
    "s_waitcnt lgkmcnt(0)\n"
    "s_lshl_b32 s98, s98, 4\n"
    "s_sub_u32 s98, s98, s97\n"
    "s_cmp_lt_i32 s98, 0\n"
    "s_cbranch_scc1 Lg8_%=\n"
    "s_waitcnt vmcnt(7)\n"
    "buffer_load_dwordx4 a[164:167], v2, s[44:47], s55 offen offset:2048 nt\n"
    "s_mul_i32 s97, s96, 20\n"
    "s_add_u32 s97, s97, s95\n"
    "Lg9_%=:\n"
    "s_memrealtime s[98:99]\n"
    "s_waitcnt lgkmcnt(0)\n"
    "s_lshl_b32 s98, s98, 4\n"
    "s_sub_u32 s98, s98, s97\n"
    "s_cmp_lt_i32 s98, 0\n"
    "s_cbranch_scc1 Lg9_%=\n"
    "s_waitcnt vmcnt(7)\n"
    "buffer_load_dwordx4 a[168:171], v2, s[44:47], s55 offen offset:2560 nt\n"
    "s_mul_i32 s97, s96, 21\n"
    "s_add_u32 s97, s97, s95\n"
    "Lg10_%=:\n"
    "s_memrealtime s[98:99]\n"
    "s_waitcnt lgkmcnt(0)\n"
    "s_lshl_b32 s98, s98, 4\n"
    "s_sub_u32 s98, s98, s97\n"
    "s_cmp_lt_i32 s98, 0\n"
    "s_cbranch_scc1 Lg10_%=\n"
    "s_waitcnt vmcnt(7)\n"
    "buffer_load_dwordx4 a[172:175], v2, s[44:47], s56 offen offset:0 nt\n"
    "s_mul_i32 s97, s96, 22\n"
    "s_add_u32 s97, s97, s95\n"
    "Lg11_%=:\n"
    "s_memrealtime s[98:99]\n"
    "s_waitcnt lgkmcnt(0)\n"
    "s_lshl_b32 s98, s98, 4\n"
    "s_sub_u32 s98, s98, s97\n"
    "s_cmp_lt_i32 s98, 0\n"
    "s_cbranch_scc1 Lg11_%=\n"
    "s_waitcnt vmcnt(7)\n"
    "buffer_load_dwordx4 a[176:179], v2, s[44:47], s56 offen offset:512 nt\n"
    "s_mul_i32 s97, s96, 23\n"
    "s_add_u32 s97, s97, s95\n"
    "Lg12_%=:\n"
    "s_memrealtime s[98:99]\n"
    "s_waitcnt lgkmcnt(0)\n"
    "s_lshl_b32 s98, s98, 4\n"
    "s_sub_u32 s98, s98, s97\n"
    "s_cmp_lt_i32 s98, 0\n"
    "s_cbranch_scc1 Lg12_%=\n"
    "s_waitcnt vmcnt(7)\n"
    "buffer_load_dwordx4 a[180:183], v2, s[44:47], s56 offen offset:2048 nt\n"
    "s_mul_i32 s97, s96, 24\n"
    "s_add_u32 s97, s97, s95\n"
    "Lg13_%=:\n"
    "s_memrealtime s[98:99]\n"
    "s_waitcnt lgkmcnt(0)\n"
    "s_lshl_b32 s98, s98, 4\n"
    "s_sub_u32 s98, s98, s97\n"
    "s_cmp_lt_i32 s98, 0\n"
    "s_cbranch_scc1 Lg13_%=\n"
    "s_waitcnt vmcnt(7)\n"
    "buffer_load_dwordx4 a[184:187], v2, s[44:47], s56 offen offset:2560 nt\n"
    "s_lshl_b32 s68, s61, 7\n"
    "s_add_u32 s68, s68, 970752\n"
    "buffer_load_dword v10, off, s[48:51], s68 sc1\n"
    "s_mul_i32 s97, s96, 25\n"
    "s_add_u32 s97, s97, s95\n"
    "Lg14_%=:\n"
    "s_memrealtime s[98:99]\n"
    "s_waitcnt lgkmcnt(0)\n"
    "s_lshl_b32 s98, s98, 4\n"
    "s_sub_u32 s98, s98, s97\n"
    "s_cmp_lt_i32 s98, 0\n"
    "s_cbranch_scc1 Lg14_%=\n"
    "s_waitcnt vmcnt(7)\n"
    "buffer_load_dwordx4 a[188:191], v2, s[44:47], s57 offen offset:0 nt\n"
    "s_mul_i32 s97, s96, 26\n"
    "s_add_u32 s97, s97, s95\n"
    "Lg15_%=:\n"
    "s_memrealtime s[98:99]\n"
    "s_waitcnt lgkmcnt(0)\n"
    "s_lshl_b32 s98, s98, 4\n"
    "s_sub_u32 s98, s98, s97\n"
    "s_cmp_lt_i32 s98, 0\n"
    "s_cbranch_scc1 Lg15_%=\n"
    "s_waitcnt vmcnt(7)\n"
    "buffer_load_dwordx4 a[192:195], v2, s[44:47], s57 offen offset:512 nt\n"
    "s_mul_i32 s97, s96, 27\n"
    "s_add_u32 s97, s97, s95\n"
    "Lg16_%=:\n"
    "s_memrealtime s[98:99]\n"
    "s_waitcnt lgkmcnt(0)\n"
    "s_lshl_b32 s98, s98, 4\n"
    "s_sub_u32 s98, s98, s97\n"
    "s_cmp_lt_i32 s98, 0\n"
    "s_cbranch_scc1 Lg16_%=\n"
    "s_waitcnt vmcnt(7)\n"
    "buffer_load_dwordx4 a[196:199], v2, s[44:47], s57 offen offset:2048 nt\n"
    "s_mul_i32 s97, s96, 28\n"
    "s_add_u32 s97, s97, s95\n"
    "Lg17_%=:\n"
    "s_memrealtime s[98:99]\n"
    "s_waitcnt lgkmcnt(0)\n"
    "s_lshl_b32 s98, s98, 4\n"
    "s_sub_u32 s98, s98, s97\n"
    "s_cmp_lt_i32 s98, 0\n"
    "s_cbranch_scc1 Lg17_%=\n"
    "s_waitcnt vmcnt(7)\n"
    "buffer_load_dwordx4 a[200:203], v2, s[44:47], s57 offen offset:2560 nt\n"
    "s_mul_i32 s97, s96, 29\n"
    "s_add_u32 s97, s97, s95\n"
    "Lg18_%=:\n"
    "s_memrealtime s[98:99]\n"
    "s_waitcnt lgkmcnt(0)\n"
    "s_lshl_b32 s98, s98, 4\n"
    "s_sub_u32 s98, s98, s97\n"
    "s_cmp_lt_i32 s98, 0\n"
    "s_cbranch_scc1 Lg18_%=\n"
    "s_waitcnt vmcnt(7)\n"
    "buffer_load_dwordx4 a[204:207], v2, s[44:47], s58 offen offset:0 nt\n"
    "s_mul_i32 s97, s96, 30\n"
    "s_add_u32 s97, s97, s95\n"
    "Lg19_%=:\n"
    "s_memrealtime s[98:99]\n"
    "s_waitcnt lgkmcnt(0)\n"
    "s_lshl_b32 s98, s98, 4\n"
    "s_sub_u32 s98, s98, s97\n"
    "s_cmp_lt_i32 s98, 0\n"
    "s_cbranch_scc1 Lg19_%=\n"
    "s_waitcnt vmcnt(7)\n"
    "buffer_load_dwordx4 a[208:211], v2, s[44:47], s58 offen offset:512 nt\n"
    "s_mul_i32 s97, s96, 31\n"
    "s_add_u32 s97, s97, s95\n"
    "Lg20_%=:\n"
    "s_memrealtime s[98:99]\n"
    "s_waitcnt lgkmcnt(0)\n"
    "s_lshl_b32 s98, s98, 4\n"
    "s_sub_u32 s98, s98, s97\n"
    "s_cmp_lt_i32 s98, 0\n"
    "s_cbranch_scc1 Lg20_%=\n"
    "s_waitcnt vmcnt(7)\n"
    "buffer_load_dwordx4 a[212:215], v2, s[44:47], s58 offen offset:2048 nt\n"
    "s_mul_i32 s97, s96, 32\n"
    "s_add_u32 s97, s97, s95\n"
    "Lg21_%=:\n"
    "s_memrealtime s[98:99]\n"
    "s_waitcnt lgkmcnt(0)\n"
    "s_lshl_b32 s98, s98, 4\n"
    "s_sub_u32 s98, s98, s97\n"
    "s_cmp_lt_i32 s98, 0\n"
    "s_cbranch_scc1 Lg21_%=\n"
    "s_waitcnt vmcnt(7)\n"
    "buffer_load_dwordx4 a[216:219], v2, s[44:47], s58 offen offset:2560 nt\n"
    "v_readfirstlane_b32 s68, v10\n"
    "s_add_u32 s68, s68, 365\n"
    "s_lshl_b32 s68, s68, 4\n"
    "s_add_u32 s68, s68, s83\n"
    "s_sub_u32 s69, s68, s95\n"
    "s_cmp_lt_u32 s69, 24000\n"
    "s_cselect_b32 s68, s68, s95\n"
    "s_cmp_ge_u32 s61, 4\n"
    "s_cselect_b32 s95, s68, s95\n"
    "s_mov_b32 s68, 970880\n"
    "buffer_load_dword v11, off, s[48:51], s68 sc1\n"
    "s_mul_i32 s97, s96, 33\n"
    "s_add_u32 s97, s97, s95\n"
    "Lg22_%=:\n"
    "s_memrealtime s[98:99]\n"
    "s_waitcnt lgkmcnt(0)\n"
    "s_lshl_b32 s98, s98, 4\n"
    "s_sub_u32 s98, s98, s97\n"
    "s_cmp_lt_i32 s98, 0\n"
    "s_cbranch_scc1 Lg22_%=\n"
    "s_waitcnt vmcnt(7)\n"
    "buffer_load_dwordx4 a[220:223], v2, s[44:47], s59 offen offset:0 nt\n"
    "s_mul_i32 s97, s96, 34\n"
    "s_add_u32 s97, s97, s95\n"
    "Lg23_%=:\n"
    "s_memrealtime s[98:99]\n"
    "s_waitcnt lgkmcnt(0)\n"
    "s_lshl_b32 s98, s98, 4\n"
    "s_sub_u32 s98, s98, s97\n"
    "s_cmp_lt_i32 s98, 0\n"
    "s_cbranch_scc1 Lg23_%=\n"
    "s_waitcnt vmcnt(7)\n"
    "buffer_load_dwordx4 a[224:227], v2, s[44:47], s59 offen offset:512 nt\n"
    "s_mul_i32 s97, s96, 35\n"
    "s_add_u32 s97, s97, s95\n"
    "Lg24_%=:\n"
    "s_memrealtime s[98:99]\n"
    "s_waitcnt lgkmcnt(0)\n"
    "s_lshl_b32 s98, s98, 4\n"
    "s_sub_u32 s98, s98, s97\n"
    "s_cmp_lt_i32 s98, 0\n"
    "s_cbranch_scc1 Lg24_%=\n"
    "s_waitcnt vmcnt(7)\n"
    "buffer_load_dwordx4 a[228:231], v2, s[44:47], s59 offen offset:2048 nt\n"
    "s_mul_i32 s97, s96, 36\n"
    "s_add_u32 s97, s97, s95\n"
    "Lg25_%=:\n"
    "s_memrealtime s[98:99]\n"
    "s_waitcnt lgkmcnt(0)\n"
    "s_lshl_b32 s98, s98, 4\n"
    "s_sub_u32 s98, s98, s97\n"
    "s_cmp_lt_i32 s98, 0\n"
    "s_cbranch_scc1 Lg25_%=\n"
    "s_waitcnt vmcnt(7)\n"
    "buffer_load_dwordx4 a[232:235], v2, s[44:47], s59 offen offset:2560 nt\n"
    "s_add_u32 s44, s44, 0x2000000\n"
    "s_addc_u32 s45, s45, 0\n"
    "s_mul_i32 s97, s96, 37\n"
    "s_add_u32 s97, s97, s95\n"
    "Lg26_%=:\n"
    "s_memrealtime s[98:99]\n"
    "s_waitcnt lgkmcnt(0)\n"
    "s_lshl_b32 s98, s98, 4\n"
    "s_sub_u32 s98, s98, s97\n"
    "s_cmp_lt_i32 s98, 0\n"
    "s_cbranch_scc1 Lg26_%=\n"
    "s_waitcnt vmcnt(7)\n"
    "buffer_load_dwordx4 a[0:3], v0, s[40:43], 0 offen offset:0 nt\n"
    "s_mul_i32 s97, s96, 38\n"
    "s_add_u32 s97, s97, s95\n"
    "Lg27_%=:\n"
    "s_memrealtime s[98:99]\n"
    "s_waitcnt lgkmcnt(0)\n"
    "s_lshl_b32 s98, s98, 4\n"
    "s_sub_u32 s98, s98, s97\n"
    "s_cmp_lt_i32 s98, 0\n"
    "s_cbranch_scc1 Lg27_%=\n"
    "s_waitcnt vmcnt(7)\n"
    "buffer_load_dwordx4 a[4:7], v0, s[40:43], 0 offen offset:1024 nt\n"
    "s_mul_i32 s97, s96, 39\n"
    "s_add_u32 s97, s97, s95\n"
    "Lg28_%=:\n"
    "s_memrealtime s[98:99]\n"
    "s_waitcnt lgkmcnt(0)\n"
    "s_lshl_b32 s98, s98, 4\n"
    "s_sub_u32 s98, s98, s97\n"
    "s_cmp_lt_i32 s98, 0\n"
    "s_cbranch_scc1 Lg28_%=\n"
    "s_waitcnt vmcnt(7)\n"
    "buffer_load_dwordx4 a[8:11], v0, s[40:43], 0 offen offset:2048 nt\n"
    "s_mul_i32 s97, s96, 40\n"
    "s_add_u32 s97, s97, s95\n"
    "Lg29_%=:\n"
    "s_memrealtime s[98:99]\n"
    "s_waitcnt lgkmcnt(0)\n"
    "s_lshl_b32 s98, s98, 4\n"
    "s_sub_u32 s98, s98, s97\n"
    "s_cmp_lt_i32 s98, 0\n"
    "s_cbranch_scc1 Lg29_%=\n"
    "s_waitcnt vmcnt(7)\n"
    "buffer_load_dwordx4 a[12:15], v0, s[40:43], 0 offen offset:3072 nt\n"
    "v_readfirstlane_b32 s68, v11\n"
    "s_add_u32 s68, s68, -20\n"
    "s_lshl_b32 s68, s68, 4\n"
    "s_add_u32 s68, s68, s83\n"
    "s_sub_u32 s69, s68, s95\n"
    "s_add_u32 s69, s69, 2400\n"
    "s_cmp_lt_u32 s69, 4800\n"
    "s_cselect_b32 s68, s68, s95\n"
    "s_cmp_eq_u32 s61, 0\n"
    "s_cselect_b32 s95, s68, s95\n"
    "s_mul_i32 s97, s96, 41\n"
    "s_add_u32 s97, s97, s95\n"
    "Lg30_%=:\n"
    "s_memrealtime s[98:99]\n"
    "s_waitcnt lgkmcnt(0)\n"
    "s_lshl_b32 s98, s98, 4\n"
    "s_sub_u32 s98, s98, s97\n"
    "s_cmp_lt_i32 s98, 0\n"
    "s_cbranch_scc1 Lg30_%=\n"
    "s_waitcnt vmcnt(7)\n"
    "buffer_load_dwordx4 a[16:19], v0, s[40:43], s52 offen offset:0 nt\n"
    "s_mul_i32 s97, s96, 42\n"
    "s_add_u32 s97, s97, s95\n"
    "Lg31_%=:\n"
    "s_memrealtime s[98:99]\n"
    "s_waitcnt lgkmcnt(0)\n"
    "s_lshl_b32 s98, s98, 4\n"
    "s_sub_u32 s98, s98, s97\n"
    "s_cmp_lt_i32 s98, 0\n"
    "s_cbranch_scc1 Lg31_%=\n"
    "s_waitcnt vmcnt(7)\n"
    "buffer_load_dwordx4 a[20:23], v0, s[40:43], s52 offen offset:1024 nt\n"
    "s_mul_i32 s97, s96, 43\n"
    "s_add_u32 s97, s97, s95\n"
    "Lg32_%=:\n"
    "s_memrealtime s[98:99]\n"
    "s_waitcnt lgkmcnt(0)\n"
    "s_lshl_b32 s98, s98, 4\n"
    "s_sub_u32 s98, s98, s97\n"
    "s_cmp_lt_i32 s98, 0\n"
    "s_cbranch_scc1 Lg32_%=\n"
    "s_waitcnt vmcnt(7)\n"
    "buffer_load_dwordx4 a[24:27], v0, s[40:43], s52 offen offset:2048 nt\n"
    "s_mul_i32 s97, s96, 44\n"
    "s_add_u32 s97, s97, s95\n"
    "Lg33_%=:\n"
    "s_memrealtime s[98:99]\n"
    "s_waitcnt lgkmcnt(0)\n"
    "s_lshl_b32 s98, s98, 4\n"
    "s_sub_u32 s98, s98, s97\n"
    "s_cmp_lt_i32 s98, 0\n"
    "s_cbranch_scc1 Lg33_%=\n"
    "s_waitcnt vmcnt(7)\n"
    "buffer_load_dwordx4 a[28:31], v0, s[40:43], s52 offen offset:3072 nt\n"
    "s_cmp_lg_u32 s61, 0\n"
    "s_cbranch_scc1 Lfl0_%=\n"
    "buffer_load_dwordx2 v[8:9], off, s[48:51], s94 sc1\n"
    "Lfl0_%=:\n"
    "s_mul_i32 s97, s96, 45\n"
    "s_add_u32 s97, s97, s95\n"
    "Lg34_%=:\n"
    "s_memrealtime s[98:99]\n"
    "s_waitcnt lgkmcnt(0)\n"
    "s_lshl_b32 s98, s98, 4\n"
    "s_sub_u32 s98, s98, s97\n"
    "s_cmp_lt_i32 s98, 0\n"
    "s_cbranch_scc1 Lg34_%=\n"
    "s_waitcnt vmcnt(7)\n"
    "buffer_load_dwordx4 a[32:35], v0, s[40:43], s53 offen offset:0 nt\n"
    "s_mul_i32 s97, s96, 46\n"
    "s_add_u32 s97, s97, s95\n"
    "Lg35_%=:\n"
    "s_memrealtime s[98:99]\n"
    "s_waitcnt lgkmcnt(0)\n"
    "s_lshl_b32 s98, s98, 4\n"
    "s_sub_u32 s98, s98, s97\n"
    "s_cmp_lt_i32 s98, 0\n"
    "s_cbranch_scc1 Lg35_%=\n"
    "s_waitcnt vmcnt(7)\n"
    "buffer_load_dwordx4 a[36:39], v0, s[40:43], s53 offen offset:1024 nt\n"
    "s_mul_i32 s97, s96, 47\n"
    "s_add_u32 s97, s97, s95\n"
    "Lg36_%=:\n"
    "s_memrealtime s[98:99]\n"
    "s_waitcnt lgkmcnt(0)\n"
    "s_lshl_b32 s98, s98, 4\n"
    "s_sub_u32 s98, s98, s97\n"
    "s_cmp_lt_i32 s98, 0\n"
    "s_cbranch_scc1 Lg36_%=\n"
    "s_waitcnt vmcnt(7)\n"
    "buffer_load_dwordx4 a[40:43], v0, s[40:43], s53 offen offset:2048 nt\n"
    "s_mul_i32 s97, s96, 48\n"
    "s_add_u32 s97, s97, s95\n"
    "Lg37_%=:\n"
    "s_memrealtime s[98:99]\n"
    "s_waitcnt lgkmcnt(0)\n"
    "s_lshl_b32 s98, s98, 4\n"
    "s_sub_u32 s98, s98, s97\n"
    "s_cmp_lt_i32 s98, 0\n"
    "s_cbranch_scc1 Lg37_%=\n"
    "s_waitcnt vmcnt(7)\n"
    "buffer_load_dwordx4 a[44:47], v0, s[40:43], s53 offen offset:3072 nt\n"
    "s_mul_i32 s97, s96, 49\n"
    "s_add_u32 s97, s97, s95\n"
    "Lg38_%=:\n"
    "s_memrealtime s[98:99]\n"
    "s_waitcnt lgkmcnt(0)\n"
    "s_lshl_b32 s98, s98, 4\n"
    "s_sub_u32 s98, s98, s97\n"
    "s_cmp_lt_i32 s98, 0\n"
    "s_cbranch_scc1 Lg38_%=\n"
    "s_waitcnt vmcnt(7)\n"
    "buffer_load_dwordx4 a[48:51], v0, s[40:43], s54 offen offset:0 nt\n"
    "s_mul_i32 s97, s96, 50\n"
    "s_add_u32 s97, s97, s95\n"
    "Lg39_%=:\n"
    "s_memrealtime s[98:99]\n"
    "s_waitcnt lgkmcnt(0)\n"
    "s_lshl_b32 s98, s98, 4\n"
    "s_sub_u32 s98, s98, s97\n"
    "s_cmp_lt_i32 s98, 0\n"
    "s_cbranch_scc1 Lg39_%=\n"
    "s_waitcnt vmcnt(7)\n"
    "buffer_load_dwordx4 a[52:55], v0, s[40:43], s54 offen offset:1024 nt\n"
    "s_mul_i32 s97, s96, 51\n"
    "s_add_u32 s97, s97, s95\n"
    "Lg40_%=:\n"
    "s_memrealtime s[98:99]\n"
    "s_waitcnt lgkmcnt(0)\n"
    "s_lshl_b32 s98, s98, 4\n"
    "s_sub_u32 s98, s98, s97\n"
    "s_cmp_lt_i32 s98, 0\n"
    "s_cbranch_scc1 Lg40_%=\n"
    "s_waitcnt vmcnt(7)\n"
    "buffer_load_dwordx4 a[56:59], v0, s[40:43], s54 offen offset:2048 nt\n"
    "s_mul_i32 s97, s96, 52\n"
    "s_add_u32 s97, s97, s95\n"
    "Lg41_%=:\n"
    "s_memrealtime s[98:99]\n"
    "s_waitcnt lgkmcnt(0)\n"
    "s_lshl_b32 s98, s98, 4\n"
    "s_sub_u32 s98, s98, s97\n"
    "s_cmp_lt_i32 s98, 0\n"
    "s_cbranch_scc1 Lg41_%=\n"
    "s_waitcnt vmcnt(7)\n"
    "buffer_load_dwordx4 a[60:63], v0, s[40:43], s54 offen offset:3072 nt\n"
    "s_cmp_lg_u32 s61, 0\n"
    "s_cbranch_scc1 Lfld_%=\n"
    "s_waitcnt vmcnt(8)\n"
    "Lflc_%=:\n"
    "v_cmp_eq_u64 s[68:69], s[92:93], v[8:9]\n"
    "s_cmp_lg_u64 s[68:69], 0\n"
    "s_nop 3\n"
    "s_cbranch_scc1 Lfld_%=\n"
    "s_sub_u32 s67, s67, 1\n"
    "s_cmp_lt_i32 s67, 0\n"
    "s_cbranch_scc1 Lfld_%=\n"
    "buffer_load_dwordx2 v[8:9], off, s[48:51], s94 sc1\n"
    "s_waitcnt vmcnt(0)\n"
    "s_branch Lflc_%=\n"
    "Lfld_%=:\n"
    "s_mul_i32 s97, s96, 53\n"
    "s_add_u32 s97, s97, s95\n"
    "Lg42_%=:\n"
    "s_memrealtime s[98:99]\n"
    "s_waitcnt lgkmcnt(0)\n"
    "s_lshl_b32 s98, s98, 4\n"
    "s_sub_u32 s98, s98, s97\n"
    "s_cmp_lt_i32 s98, 0\n"
    "s_cbranch_scc1 Lg42_%=\n"
    "s_waitcnt vmcnt(7)\n"
    "buffer_load_dwordx4 a[64:67], v0, s[40:43], s55 offen offset:0 nt\n"
    "s_mul_i32 s97, s96, 54\n"
    "s_add_u32 s97, s97, s95\n"
    "Lg43_%=:\n"
    "s_memrealtime s[98:99]\n"
    "s_waitcnt lgkmcnt(0)\n"
    "s_lshl_b32 s98, s98, 4\n"
    "s_sub_u32 s98, s98, s97\n"
    "s_cmp_lt_i32 s98, 0\n"
    "s_cbranch_scc1 Lg43_%=\n"
    "s_waitcnt vmcnt(7)\n"
    "buffer_load_dwordx4 a[68:71], v0, s[40:43], s55 offen offset:1024 nt\n"
    "s_mul_i32 s97, s96, 55\n"
    "s_add_u32 s97, s97, s95\n"
    "Lg44_%=:\n"
    "s_memrealtime s[98:99]\n"
    "s_waitcnt lgkmcnt(0)\n"
    "s_lshl_b32 s98, s98, 4\n"
    "s_sub_u32 s98, s98, s97\n"
    "s_cmp_lt_i32 s98, 0\n"
    "s_cbranch_scc1 Lg44_%=\n"
    "s_waitcnt vmcnt(7)\n"
    "buffer_load_dwordx4 a[72:75], v0, s[40:43], s55 offen offset:2048 nt\n"
    "s_mul_i32 s97, s96, 56\n"
    "s_add_u32 s97, s97, s95\n"
    "Lg45_%=:\n"
    "s_memrealtime s[98:99]\n"
    "s_waitcnt lgkmcnt(0)\n"
    "s_lshl_b32 s98, s98, 4\n"
    "s_sub_u32 s98, s98, s97\n"
    "s_cmp_lt_i32 s98, 0\n"
    "s_cbranch_scc1 Lg45_%=\n"
    "s_waitcnt vmcnt(7)\n"
    "buffer_load_dwordx4 a[76:79], v0, s[40:43], s55 offen offset:3072 nt\n"
    "s_mul_i32 s97, s96, 57\n"
    "s_add_u32 s97, s97, s95\n"
    "Lg46_%=:\n"
    "s_memrealtime s[98:99]\n"
    "s_waitcnt lgkmcnt(0)\n"
    "s_lshl_b32 s98, s98, 4\n"
    "s_sub_u32 s98, s98, s97\n"
    "s_cmp_lt_i32 s98, 0\n"
    "s_cbranch_scc1 Lg46_%=\n"
    "s_waitcnt vmcnt(7)\n"
    "buffer_load_dwordx4 a[80:83], v0, s[40:43], s56 offen offset:0 nt\n"
    "s_mul_i32 s97, s96, 58\n"
    "s_add_u32 s97, s97, s95\n"
    "Lg47_%=:\n"
    "s_memrealtime s[98:99]\n"
    "s_waitcnt lgkmcnt(0)\n"
    "s_lshl_b32 s98, s98, 4\n"
    "s_sub_u32 s98, s98, s97\n"
    "s_cmp_lt_i32 s98, 0\n"
    "s_cbranch_scc1 Lg47_%=\n"
    "s_waitcnt vmcnt(7)\n"
    "buffer_load_dwordx4 a[84:87], v0, s[40:43], s56 offen offset:1024 nt\n"
    "s_mul_i32 s97, s96, 59\n"
    "s_add_u32 s97, s97, s95\n"
    "Lg48_%=:\n"
    "s_memrealtime s[98:99]\n"
    "s_waitcnt lgkmcnt(0)\n"
    "s_lshl_b32 s98, s98, 4\n"
    "s_sub_u32 s98, s98, s97\n"
    "s_cmp_lt_i32 s98, 0\n"
    "s_cbranch_scc1 Lg48_%=\n"
    "s_waitcnt vmcnt(7)\n"
    "buffer_load_dwordx4 a[88:91], v0, s[40:43], s56 offen offset:2048 nt\n"
    "s_mul_i32 s97, s96, 60\n"
    "s_add_u32 s97, s97, s95\n"
    "Lg49_%=:\n"
    "s_memrealtime s[98:99]\n"
    "s_waitcnt lgkmcnt(0)\n"
    "s_lshl_b32 s98, s98, 4\n"
    "s_sub_u32 s98, s98, s97\n"
    "s_cmp_lt_i32 s98, 0\n"
    "s_cbranch_scc1 Lg49_%=\n"
    "s_waitcnt vmcnt(7)\n"
    "buffer_load_dwordx4 a[92:95], v0, s[40:43], s56 offen offset:3072 nt\n"
    "s_mul_i32 s97, s96, 61\n"
    "s_add_u32 s97, s97, s95\n"
    "Lg50_%=:\n"
    "s_memrealtime s[98:99]\n"
    "s_waitcnt lgkmcnt(0)\n"
    "s_lshl_b32 s98, s98, 4\n"
    "s_sub_u32 s98, s98, s97\n"
    "s_cmp_lt_i32 s98, 0\n"
    "s_cbranch_scc1 Lg50_%=\n"
    "s_waitcnt vmcnt(7)\n"
    "buffer_load_dwordx4 a[96:99], v0, s[40:43], s57 offen offset:0 nt\n"
    "s_mul_i32 s97, s96, 62\n"
    "s_add_u32 s97, s97, s95\n"
    "Lg51_%=:\n"
    "s_memrealtime s[98:99]\n"
    "s_waitcnt lgkmcnt(0)\n"
    "s_lshl_b32 s98, s98, 4\n"
    "s_sub_u32 s98, s98, s97\n"
    "s_cmp_lt_i32 s98, 0\n"
    "s_cbranch_scc1 Lg51_%=\n"
    "s_waitcnt vmcnt(7)\n"
    "buffer_load_dwordx4 a[100:103], v0, s[40:43], s57 offen offset:1024 nt\n"
    "s_add_u32 s68, s61, 7\n"
    "s_lshl_b32 s68, s68, 13\n"
    "s_add_u32 s68, s68, s65\n"
    "v_lshrrev_b32 v3, 6, v1\n"
    "v_lshlrev_b32 v3, 4, v3\n"
    "buffer_load_dwordx4 v[4:7], v3, s[48:51], s68 offen sc1\n"
    "s_mul_i32 s97, s96, 63\n"
    "s_add_u32 s97, s97, s95\n"
    "Lg52_%=:\n"
    "s_memrealtime s[98:99]\n"
    "s_waitcnt lgkmcnt(0)\n"
    "s_lshl_b32 s98, s98, 4\n"
    "s_sub_u32 s98, s98, s97\n"
    "s_cmp_lt_i32 s98, 0\n"
    "s_cbranch_scc1 Lg52_%=\n"
    "s_waitcnt vmcnt(7)\n"
    "buffer_load_dwordx4 a[104:107], v0, s[40:43], s57 offen offset:2048 nt\n"
    "s_mul_i32 s97, s96, 64\n"
    "s_add_u32 s97, s97, s95\n"
    "Lg53_%=:\n"
    "s_memrealtime s[98:99]\n"
    "s_waitcnt lgkmcnt(0)\n"
    "s_lshl_b32 s98, s98, 4\n"
    "s_sub_u32 s98, s98, s97\n"
    "s_cmp_lt_i32 s98, 0\n"
    "s_cbranch_scc1 Lg53_%=\n"
    "s_waitcnt vmcnt(7)\n"
    "buffer_load_dwordx4 a[108:111], v0, s[40:43], s57 offen offset:3072 nt\n"
    "s_mul_i32 s97, s96, 65\n"
    "s_add_u32 s97, s97, s95\n"
    "Lg54_%=:\n"
    "s_memrealtime s[98:99]\n"
    "s_waitcnt lgkmcnt(0)\n"
    "s_lshl_b32 s98, s98, 4\n"
    "s_sub_u32 s98, s98, s97\n"
    "s_cmp_lt_i32 s98, 0\n"
    "s_cbranch_scc1 Lg54_%=\n"
    "s_waitcnt vmcnt(7)\n"
    "buffer_load_dwordx4 a[112:115], v0, s[40:43], s58 offen offset:0 nt\n"
    "s_mul_i32 s97, s96, 66\n"
    "s_add_u32 s97, s97, s95\n"
    "Lg55_%=:\n"
    "s_memrealtime s[98:99]\n"
    "s_waitcnt lgkmcnt(0)\n"
    "s_lshl_b32 s98, s98, 4\n"
    "s_sub_u32 s98, s98, s97\n"
    "s_cmp_lt_i32 s98, 0\n"
    "s_cbranch_scc1 Lg55_%=\n"
    "s_waitcnt vmcnt(7)\n"
    "buffer_load_dwordx4 a[116:119], v0, s[40:43], s58 offen offset:1024 nt\n"
    "s_mul_i32 s97, s96, 67\n"
    "s_add_u32 s97, s97, s95\n"
    "Lg56_%=:\n"
    "s_memrealtime s[98:99]\n"
    "s_waitcnt lgkmcnt(0)\n"
    "s_lshl_b32 s98, s98, 4\n"
    "s_sub_u32 s98, s98, s97\n"
    "s_cmp_lt_i32 s98, 0\n"
    "s_cbranch_scc1 Lg56_%=\n"
    "s_waitcnt vmcnt(7)\n"
    "buffer_load_dwordx4 a[120:123], v0, s[40:43], s58 offen offset:2048 nt\n"
    "s_waitcnt vmcnt(5)\n"
    "s_cmp_eq_u32 s61, 0\n"
    "s_cbranch_scc1 Lqbd_%=\n"
    "Lqbc_%=:\n"
    "v_max3_u32 v3, v4, v5, v6\n"
    "v_max_u32 v3, v3, v7\n"
    "v_cmp_eq_u32 vcc, -1, v3\n"
    "s_cbranch_vccz Lqbd_%=\n"
    "s_nop 3\n"
    "Lqbr_%=:\n"
    "s_sub_u32 s67, s67, 1\n"
    "s_cmp_lt_i32 s67, 0\n"
    "s_cbranch_scc1 Lqbd_%=\n"
    "s_add_u32 s68, s61, 7\n"
    "s_lshl_b32 s68, s68, 13\n"
    "s_add_u32 s68, s68, s65\n"
    "v_lshrrev_b32 v3, 6, v1\n"
    "v_lshlrev_b32 v3, 4, v3\n"
    "buffer_load_dwordx4 v[4:7], v3, s[48:51], s68 offen sc1\n"
    "s_memrealtime s[98:99]\n"
    "s_mul_i32 s97, s96, 68\n"
    "s_waitcnt lgkmcnt(0)\n"
    "s_lshl_b32 s98, s98, 4\n"
    "s_sub_u32 s98, s98, s97\n"
    "s_sub_u32 s97, s98, s95\n"
    "s_cmp_gt_i32 s97, 0\n"
    "s_cselect_b32 s95, s98, s95\n"
    "s_waitcnt vmcnt(0)\n"
    "s_branch Lqbc_%=\n"
    "Lqbd_%=:\n"
    "v_cndmask_b32 v8, 0, v4, s[70:71]\n"
    "v_cndmask_b32 v9, 0, v5, s[70:71]\n"
    "v_cndmask_b32 v10, 0, v6, s[70:71]\n"
    "v_cndmask_b32 v11, 0, v7, s[70:71]\n"
    "v_cndmask_b32 v12, 0, v4, s[72:73]\n"
    "v_cndmask_b32 v13, 0, v5, s[72:73]\n"
    "v_cndmask_b32 v14, 0, v6, s[72:73]\n"
    "v_cndmask_b32 v15, 0, v7, s[72:73]\n"
    "v_cndmask_b32 v16, 0, v4, s[74:75]\n"
    "v_cndmask_b32 v17, 0, v5, s[74:75]\n"
    "v_cndmask_b32 v18, 0, v6, s[74:75]\n"
    "v_cndmask_b32 v19, 0, v7, s[74:75]\n"
    "v_cndmask_b32 v4, 0, v4, s[76:77]\n"
    "v_cndmask_b32 v5, 0, v5, s[76:77]\n"
    "v_cndmask_b32 v6, 0, v6, s[76:77]\n"
    "v_cndmask_b32 v7, 0, v7, s[76:77]\n"
    "v_add_u32 v3, s64, v1\n"
    "v_mfma_f32_16x16x32_bf16 v[20:23], a[240:243], v[8:11], 0\n"
    "ds_read_b128 a[240:243], v3 offset:22528\n"
    "v_mfma_f32_16x16x32_bf16 v[20:23], a[244:247], v[12:15], v[20:23]\n"
    "ds_read_b128 a[244:247], v3 offset:23552\n"
    "v_mfma_f32_16x16x32_bf16 v[20:23], a[248:251], v[16:19], v[20:23]\n"
    "ds_read_b128 a[248:251], v3 offset:24576\n"
    "v_mfma_f32_16x16x32_bf16 v[20:23], a[252:255], v[4:7], v[20:23]\n"
    "ds_read_b128 a[252:255], v3 offset:25600\n"
    "buffer_load_dwordx4 a[124:127], v0, s[40:43], s58 offen offset:3072 nt\n"
    "v_mfma_f32_16x16x32_bf16 v[24:27], v[40:43], v[8:11], 0\n"
    "ds_read_b128 v[40:43], v3 offset:26624\n"
    "v_mfma_f32_16x16x32_bf16 v[24:27], v[44:47], v[12:15], v[24:27]\n"
    "ds_read_b128 v[44:47], v3 offset:27648\n"
    "v_mfma_f32_16x16x32_bf16 v[24:27], v[48:51], v[16:19], v[24:27]\n"
    "ds_read_b128 v[48:51], v3 offset:28672\n"
    "v_mfma_f32_16x16x32_bf16 v[24:27], v[52:55], v[4:7], v[24:27]\n"
    "ds_read_b128 v[52:55], v3 offset:29696\n"
    "v_mfma_f32_16x16x32_bf16 v[28:31], v[56:59], v[8:11], 0\n"
    "ds_read_b128 v[56:59], v3 offset:30720\n"
    "v_add_f32_dpp v37, v21, v20 row_ror:12 row_mask:0xf bank_mask:0x1\n"
    "v_mfma_f32_16x16x32_bf16 v[28:31], v[60:63], v[12:15], v[28:31]\n"
    "ds_read_b128 v[60:63], v3 offset:31744\n"
    "v_add_f32_dpp v37, v22, v37 row_ror:8 row_mask:0xf bank_mask:0x1\n"
    "v_mfma_f32_16x16x32_bf16 v[28:31], v[64:67], v[16:19], v[28:31]\n"
    "ds_read_b128 v[64:67], v3 offset:32768\n"
    "v_add_f32_dpp v37, v23, v37 row_ror:4 row_mask:0xf bank_mask:0x1\n"
    "v_mfma_f32_16x16x32_bf16 v[28:31], v[68:71], v[4:7], v[28:31]\n"
    "ds_read_b128 v[68:71], v3 offset:33792\n"
    "v_mfma_f32_16x16x32_bf16 v[20:23], v[72:75], v[8:11], 0\n"
    "ds_read_b128 v[72:75], v3 offset:34816\n"
    "buffer_load_dwordx4 a[128:131], v0, s[40:43], s59 offen offset:0 nt\n"
    "v_add_f32_dpp v37, v26, v25 row_ror:12 row_mask:0xf bank_mask:0x2\n"
    "v_mfma_f32_16x16x32_bf16 v[20:23], v[76:79], v[12:15], v[20:23]\n"
    "ds_read_b128 v[76:79], v3 offset:35840\n"
    "v_add_f32_dpp v37, v27, v37 row_ror:8 row_mask:0xf bank_mask:0x2\n"
    "v_mfma_f32_16x16x32_bf16 v[20:23], v[80:83], v[16:19], v[20:23]\n"
    "ds_read_b128 v[80:83], v3 offset:36864\n"
    "v_add_f32_dpp v37, v24, v37 row_ror:4 row_mask:0xf bank_mask:0x2\n"
    "v_mfma_f32_16x16x32_bf16 v[20:23], v[84:87], v[4:7], v[20:23]\n"
    "v_mfma_f32_16x16x32_bf16 v[24:27], v[88:91], v[8:11], 0\n"
    "v_add_f32_dpp v37, v31, v30 row_ror:12 row_mask:0xf bank_mask:0x4\n"
    "v_mfma_f32_16x16x32_bf16 v[24:27], v[92:95], v[12:15], v[24:27]\n"
    "s_nop 0\n"
    "v_add_f32_dpp v37, v28, v37 row_ror:8 row_mask:0xf bank_mask:0x4\n"
    "v_mfma_f32_16x16x32_bf16 v[24:27], v[96:99], v[16:19], v[24:27]\n"
    "s_nop 0\n"
    "v_add_f32_dpp v37, v29, v37 row_ror:4 row_mask:0xf bank_mask:0x4\n"
    "v_mfma_f32_16x16x32_bf16 v[24:27], v[100:103], v[4:7], v[24:27]\n"
    "s_waitcnt lgkmcnt(0)\n"
    "v_mfma_f32_16x16x32_bf16 v[28:31], v[104:107], v[8:11], 0\n"
    "v_add_f32_dpp v37, v20, v23 row_ror:12 row_mask:0xf bank_mask:0x8\n"
    "s_nop 1\n"
    "v_add_f32_dpp v37, v21, v37 row_ror:8 row_mask:0xf bank_mask:0x8\n"
    "s_nop 1\n"
    "v_add_f32_dpp v37, v22, v37 row_ror:4 row_mask:0xf bank_mask:0x8\n"
    "v_mfma_f32_16x16x32_bf16 v[20:23], a[240:243], v[8:11], 0\n"
    "buffer_load_dwordx4 a[132:135], v0, s[40:43], s59 offen offset:1024 nt\n"
    "v_add_f32_dpp v38, v25, v24 row_ror:12 row_mask:0xf bank_mask:0x1\n"
    "v_mfma_f32_16x16x32_bf16 v[20:23], a[244:247], v[12:15], v[20:23]\n"
    "s_nop 0\n"
    "v_add_f32_dpp v38, v26, v38 row_ror:8 row_mask:0xf bank_mask:0x1\n"
    "v_mfma_f32_16x16x32_bf16 v[20:23], a[248:251], v[16:19], v[20:23]\n"
    "s_nop 0\n"
    "v_add_f32_dpp v38, v27, v38 row_ror:4 row_mask:0xf bank_mask:0x1\n"
    "v_mfma_f32_16x16x32_bf16 v[20:23], a[252:255], v[4:7], v[20:23]\n"
    "v_mfma_f32_16x16x32_bf16 v[24:27], v[40:43], v[8:11], 0\n"
    "v_add_f32_dpp v38, v30, v29 row_ror:12 row_mask:0xf bank_mask:0x2\n"
    "v_mfma_f32_16x16x32_bf16 v[24:27], v[44:47], v[12:15], v[24:27]\n"
    "s_nop 0\n"
    "v_add_f32_dpp v38, v31, v38 row_ror:8 row_mask:0xf bank_mask:0x2\n"
    "v_mfma_f32_16x16x32_bf16 v[24:27], v[48:51], v[16:19], v[24:27]\n"
    "s_nop 0\n"
    "v_add_f32_dpp v38, v28, v38 row_ror:4 row_mask:0xf bank_mask:0x2\n"
    "v_mfma_f32_16x16x32_bf16 v[24:27], v[52:55], v[4:7], v[24:27]\n"
    "v_mfma_f32_16x16x32_bf16 v[28:31], v[56:59], v[8:11], 0\n"
    "v_add_f32_dpp v36, v21, v20 row_ror:12 row_mask:0xf bank_mask:0x1\n"
    "v_mfma_f32_16x16x32_bf16 v[28:31], v[60:63], v[12:15], v[28:31]\n"
    "buffer_load_dwordx4 a[136:139], v0, s[40:43], s59 offen offset:2048 nt\n"
    "v_add_f32_dpp v36, v22, v36 row_ror:8 row_mask:0xf bank_mask:0x1\n"
    "v_mfma_f32_16x16x32_bf16 v[28:31], v[64:67], v[16:19], v[28:31]\n"
    "s_nop 0\n"
    "v_add_f32_dpp v36, v23, v36 row_ror:4 row_mask:0xf bank_mask:0x1\n"
    "v_mfma_f32_16x16x32_bf16 v[28:31], v[68:71], v[4:7], v[28:31]\n"
    "v_mfma_f32_16x16x32_bf16 v[20:23], v[72:75], v[8:11], 0\n"
    "v_add_f32_dpp v36, v26, v25 row_ror:12 row_mask:0xf bank_mask:0x2\n"
    "v_mfma_f32_16x16x32_bf16 v[20:23], v[76:79], v[12:15], v[20:23]\n"
    "s_nop 0\n"
    "v_add_f32_dpp v36, v27, v36 row_ror:8 row_mask:0xf bank_mask:0x2\n"
    "v_mfma_f32_16x16x32_bf16 v[20:23], v[80:83], v[16:19], v[20:23]\n"
    "s_nop 0\n"
    "v_add_f32_dpp v36, v24, v36 row_ror:4 row_mask:0xf bank_mask:0x2\n"
    "v_mfma_f32_16x16x32_bf16 v[20:23], a[236:239], v[4:7], v[20:23]\n"
    "s_nop 0\n"
    "v_add_f32_dpp v36, v31, v30 row_ror:12 row_mask:0xf bank_mask:0x4\n"
    "s_nop 1\n"
    "v_add_f32_dpp v36, v28, v36 row_ror:8 row_mask:0xf bank_mask:0x4\n"
    "s_nop 1\n"
    "v_add_f32_dpp v36, v29, v36 row_ror:4 row_mask:0xf bank_mask:0x4\n"
    "s_nop 1\n"
    "v_add_f32_dpp v36, v20, v23 row_ror:12 row_mask:0xf bank_mask:0x8\n"
    "s_nop 1\n"
    "v_add_f32_dpp v36, v21, v36 row_ror:8 row_mask:0xf bank_mask:0x8\n"
    "s_nop 1\n"
    "v_add_f32_dpp v36, v22, v36 row_ror:4 row_mask:0xf bank_mask:0x8\n"
    "v_cvt_pk_bf16_f32 v8, v36, v37\n"
    "v_cvt_pk_bf16_f32 v9, v38, v38\n"
    "v_lshlrev_b32 v36, 16, v8\n"
    "v_and_b32 v37, s87, v8\n"
    "v_and_b32 v38, s87, v9\n"
    "v_mul_f32 v36, s84, v36\n"
    "v_mul_f32 v37, s84, v37\n"
    "v_mul_f32 v38, s84, v38\n"
    "v_cvt_pk_bf16_f32 v8, v36, v37\n"
    "v_cvt_pk_bf16_f32 v9, v38, v38\n"
    "v_mov_b32 v10, s86\n"
    "v_lshlrev_b32 v36, 16, v8\n"
    "v_and_b32 v37, s87, v8\n"
    "v_and_b32 v38, s87, v9\n"
    "v_cndmask_b32 v38, v10, v38, s[78:79]\n"
    "v_max3_f32 v8, v36, v37, v38\n"
    "s_add_u32 s68, s61, 0\n"
    "s_lshl_b32 s68, s68, 13\n"
    "s_add_u32 s68, s68, s65\n"
    "v_lshrrev_b32 v3, 6, v1\n"
    "v_lshlrev_b32 v3, 4, v3\n"
    "buffer_load_dwordx4 v[4:7], v3, s[48:51], s68 offen sc1\n"
    "buffer_load_dwordx4 a[140:143], v0, s[40:43], s59 offen offset:3072 nt\n"
    "v_max_f32_dpp v8, v8, v8 row_ror:8 row_mask:0xf bank_mask:0xf\n"
    "s_nop 1\n"
    "v_max_f32_dpp v8, v8, v8 row_ror:4 row_mask:0xf bank_mask:0xf\n"
    "s_nop 1\n"
    "v_max_f32_dpp v8, v8, v8 row_ror:2 row_mask:0xf bank_mask:0xf\n"
    "s_nop 1\n"
    "v_max_f32_dpp v9, v8, v8 row_ror:1 row_mask:0xf bank_mask:0xf\n"
    "v_max_f32_dpp v8, v8, v8 row_ror:1 row_mask:0xf bank_mask:0xf\n"
    "s_nop 1\n"
    "v_permlane16_swap_b32 v8, v9\n"
    "v_max_f32 v10, v8, v9\n"
    "v_max_f32 v8, v8, v9\n"
    "s_nop 1\n"
    "v_permlane32_swap_b32 v8, v10\n"
    "v_max_f32 v8, v8, v10\n"
    "v_mul_f32 v16, s85, v8\n"
    "v_fma_f32 v38, v38, s85, -v16\n"
    "v_fma_f32 v36, v36, s85, -v16\n"
    "v_fma_f32 v37, v37, s85, -v16\n"
    "v_exp_f32 v38, v38\n"
    "v_exp_f32 v36, v36\n"
    "v_exp_f32 v37, v37\n"
    "v_add_f32 v17, v38, v36\n"
    "v_cvt_pk_bf16_f32 v38, v38, v38\n"
    "v_add_f32 v17, v17, v37\n"
    "v_cvt_pk_bf16_f32 v36, v36, v36\n"
    "v_cvt_pk_bf16_f32 v37, v37, v37\n"
    "v_and_b32_dpp v19, v38, v35 row_newbcast:4 row_mask:0xf bank_mask:0xf\n"
    "s_mov_b32 vcc_lo, 0x3030303\n"
    "s_mov_b32 vcc_hi, 0x3030303\n"
    "v_cndmask_b32 v8, 0, v19, vcc\n"
    "s_mov_b32 vcc_lo, 0xc0c0c0c\n"
    "s_mov_b32 vcc_hi, 0xc0c0c0c\n"
    "v_cndmask_b32 v9, 0, v19, vcc\n"
    "s_mov_b32 vcc_lo, 0x30303030\n"
    "s_mov_b32 vcc_hi, 0x30303030\n"
    "v_cndmask_b32 v10, 0, v19, vcc\n"
    "s_mov_b32 vcc_lo, 0xc0c0c0c0\n"
    "s_mov_b32 vcc_hi, 0xc0c0c0c0\n"
    "v_cndmask_b32 v11, 0, v19, vcc\n"
    "v_and_b32_dpp v12, v36, v32 row_newbcast:0 row_mask:0xf bank_mask:0xf\n"
    "v_and_b32_dpp v13, v36, v33 row_newbcast:0 row_mask:0xf bank_mask:0xf\n"
    "v_and_b32_dpp v14, v36, v32 row_newbcast:1 row_mask:0xf bank_mask:0xf\n"
    "v_and_b32_dpp v15, v36, v33 row_newbcast:1 row_mask:0xf bank_mask:0xf\n"
    "v_mfma_f32_16x16x32_bf16 v[28:31], v[8:11], v[252:255], 0\n"
    "v_and_b32_dpp v8, v36, v32 row_newbcast:2 row_mask:0xf bank_mask:0xf\n"
    "v_and_b32_dpp v9, v36, v33 row_newbcast:2 row_mask:0xf bank_mask:0xf\n"
    "v_and_b32_dpp v10, v36, v32 row_newbcast:3 row_mask:0xf bank_mask:0xf\n"
    "v_and_b32_dpp v11, v36, v33 row_newbcast:3 row_mask:0xf bank_mask:0xf\n"
    "v_smfmac_f32_16x16x64_bf16 v[28:31], v[12:15], v[108:115], v34\n"
    "v_add_f32_dpp v17, v17, v17 row_ror:8 row_mask:0xf bank_mask:0xf\n"
    "v_and_b32_dpp v12, v36, v32 row_newbcast:4 row_mask:0xf bank_mask:0xf\n"
    "v_and_b32_dpp v13, v36, v33 row_newbcast:4 row_mask:0xf bank_mask:0xf\n"
    "v_and_b32_dpp v14, v36, v32 row_newbcast:5 row_mask:0xf bank_mask:0xf\n"
    "v_and_b32_dpp v15, v36, v33 row_newbcast:5 row_mask:0xf bank_mask:0xf\n"
    "v_smfmac_f32_16x16x64_bf16 v[28:31], v[8:11], v[116:123], v34\n"
    "v_add_f32_dpp v17, v17, v17 row_ror:4 row_mask:0xf bank_mask:0xf\n"
    "buffer_load_dwordx4 a[144:147], v0, s[40:43], s60 offen offset:0 nt\n"
    "s_add_u32 s40, s40, 0x2000000\n"
    "s_addc_u32 s41, s41, 0\n"
    "s_mul_i32 s97, s96, 74\n"
    "s_add_u32 s95, s95, s97\n"
    "s_mov_b32 s96, 200\n"
    "s_cmp_eq_u32 s61, 0\n"
    "s_cselect_b32 s96, 208, s96\n"
    "v_and_b32_dpp v8, v36, v32 row_newbcast:6 row_mask:0xf bank_mask:0xf\n"
    "v_and_b32_dpp v9, v36, v33 row_newbcast:6 row_mask:0xf bank_mask:0xf\n"
    "v_and_b32_dpp v10, v36, v32 row_newbcast:7 row_mask:0xf bank_mask:0xf\n"
    "v_and_b32_dpp v11, v36, v33 row_newbcast:7 row_mask:0xf bank_mask:0xf\n"
    "v_smfmac_f32_16x16x64_bf16 v[28:31], v[12:15], v[124:131], v34\n"
    "v_add_f32_dpp v17, v17, v17 row_ror:2 row_mask:0xf bank_mask:0xf\n"
    "v_and_b32_dpp v12, v36, v32 row_newbcast:8 row_mask:0xf bank_mask:0xf\n"
    "v_and_b32_dpp v13, v36, v33 row_newbcast:8 row_mask:0xf bank_mask:0xf\n"
    "v_and_b32_dpp v14, v36, v32 row_newbcast:9 row_mask:0xf bank_mask:0xf\n"
    "v_and_b32_dpp v15, v36, v33 row_newbcast:9 row_mask:0xf bank_mask:0xf\n"
    "v_smfmac_f32_16x16x64_bf16 v[28:31], v[8:11], v[132:139], v34\n"
    "v_add_f32_dpp v17, v17, v17 row_ror:1 row_mask:0xf bank_mask:0xf\n"
    "v_and_b32_dpp v8, v36, v32 row_newbcast:10 row_mask:0xf bank_mask:0xf\n"
    "v_and_b32_dpp v9, v36, v33 row_newbcast:10 row_mask:0xf bank_mask:0xf\n"
    "v_and_b32_dpp v10, v36, v32 row_newbcast:11 row_mask:0xf bank_mask:0xf\n"
    "v_and_b32_dpp v11, v36, v33 row_newbcast:11 row_mask:0xf bank_mask:0xf\n"
    "v_smfmac_f32_16x16x64_bf16 v[28:31], v[12:15], v[140:147], v34\n"
    "v_mov_b32 v18, v17\n"
    "v_and_b32_dpp v12, v36, v32 row_newbcast:12 row_mask:0xf bank_mask:0xf\n"
    "v_and_b32_dpp v13, v36, v33 row_newbcast:12 row_mask:0xf bank_mask:0xf\n"
    "v_and_b32_dpp v14, v36, v32 row_newbcast:13 row_mask:0xf bank_mask:0xf\n"
    "v_and_b32_dpp v15, v36, v33 row_newbcast:13 row_mask:0xf bank_mask:0xf\n"
    "v_smfmac_f32_16x16x64_bf16 v[28:31], v[8:11], v[148:155], v34\n"
    "v_permlane16_swap_b32 v17, v18\n"
    "v_add_f32 v17, v17, v18\n"
    "v_and_b32_dpp v8, v36, v32 row_newbcast:14 row_mask:0xf bank_mask:0xf\n"
    "v_and_b32_dpp v9, v36, v33 row_newbcast:14 row_mask:0xf bank_mask:0xf\n"
    "v_and_b32_dpp v10, v36, v32 row_newbcast:15 row_mask:0xf bank_mask:0xf\n"
    "v_and_b32_dpp v11, v36, v33 row_newbcast:15 row_mask:0xf bank_mask:0xf\n"
    "v_smfmac_f32_16x16x64_bf16 v[28:31], v[12:15], v[156:163], v34\n"
    "v_mov_b32 v18, v17\n"
    "v_and_b32_dpp v12, v37, v32 row_newbcast:0 row_mask:0xf bank_mask:0xf\n"
    "v_and_b32_dpp v13, v37, v33 row_newbcast:0 row_mask:0xf bank_mask:0xf\n"
    "v_and_b32_dpp v14, v37, v32 row_newbcast:1 row_mask:0xf bank_mask:0xf\n"
    "v_and_b32_dpp v15, v37, v33 row_newbcast:1 row_mask:0xf bank_mask:0xf\n"
    "v_smfmac_f32_16x16x64_bf16 v[28:31], v[8:11], v[164:171], v34\n"
    "v_permlane32_swap_b32 v17, v18\n"
    "v_add_f32 v17, v17, v18\n"
    "s_add_u32 m0, s64, 22528\n"
    "s_nop 0\n"
    "buffer_load_dwordx4 v0, s[40:43], 0 offen offset:0 nt lds\n"
    "v_and_b32_dpp v8, v37, v32 row_newbcast:2 row_mask:0xf bank_mask:0xf\n"
    "v_and_b32_dpp v9, v37, v33 row_newbcast:2 row_mask:0xf bank_mask:0xf\n"
    "v_and_b32_dpp v10, v37, v32 row_newbcast:3 row_mask:0xf bank_mask:0xf\n"
    "v_and_b32_dpp v11, v37, v33 row_newbcast:3 row_mask:0xf bank_mask:0xf\n"
    "v_smfmac_f32_16x16x64_bf16 v[28:31], v[12:15], v[172:179], v34\n"
    "v_mov_b32 v18, s82\n"
    "s_mov_b64 exec, 1\n"
    "ds_write_b32 v18, v16 offset:8384\n"
    "ds_write_b32 v18, v17 offset:8400\n"
    "s_mov_b64 exec, -1\n"
    "v_and_b32_dpp v12, v37, v32 row_newbcast:4 row_mask:0xf bank_mask:0xf\n"
    "v_and_b32_dpp v13, v37, v33 row_newbcast:4 row_mask:0xf bank_mask:0xf\n"
    "v_and_b32_dpp v14, v37, v32 row_newbcast:5 row_mask:0xf bank_mask:0xf\n"
    "v_and_b32_dpp v15, v37, v33 row_newbcast:5 row_mask:0xf bank_mask:0xf\n"
    "v_smfmac_f32_16x16x64_bf16 v[28:31], v[8:11], v[180:187], v34\n"
    "v_and_b32_dpp v8, v37, v32 row_newbcast:6 row_mask:0xf bank_mask:0xf\n"
    "v_and_b32_dpp v9, v37, v33 row_newbcast:6 row_mask:0xf bank_mask:0xf\n"
    "v_and_b32_dpp v10, v37, v32 row_newbcast:7 row_mask:0xf bank_mask:0xf\n"
    "v_and_b32_dpp v11, v37, v33 row_newbcast:7 row_mask:0xf bank_mask:0xf\n"
    "v_smfmac_f32_16x16x64_bf16 v[28:31], v[12:15], v[188:195], v34\n"
    "v_and_b32_dpp v12, v37, v32 row_newbcast:8 row_mask:0xf bank_mask:0xf\n"
    "v_and_b32_dpp v13, v37, v33 row_newbcast:8 row_mask:0xf bank_mask:0xf\n"
    "v_and_b32_dpp v14, v37, v32 row_newbcast:9 row_mask:0xf bank_mask:0xf\n"
    "v_and_b32_dpp v15, v37, v33 row_newbcast:9 row_mask:0xf bank_mask:0xf\n"
    "v_smfmac_f32_16x16x64_bf16 v[28:31], v[8:11], v[196:203], v34\n"
    "v_and_b32_dpp v8, v37, v32 row_newbcast:10 row_mask:0xf bank_mask:0xf\n"
    "v_and_b32_dpp v9, v37, v33 row_newbcast:10 row_mask:0xf bank_mask:0xf\n"
    "v_and_b32_dpp v10, v37, v32 row_newbcast:11 row_mask:0xf bank_mask:0xf\n"
    "v_and_b32_dpp v11, v37, v33 row_newbcast:11 row_mask:0xf bank_mask:0xf\n"
    "v_smfmac_f32_16x16x64_bf16 v[28:31], v[12:15], v[204:211], v34\n"
    "v_and_b32_dpp v12, v37, v32 row_newbcast:12 row_mask:0xf bank_mask:0xf\n"
    "v_and_b32_dpp v13, v37, v33 row_newbcast:12 row_mask:0xf bank_mask:0xf\n"
    "v_and_b32_dpp v14, v37, v32 row_newbcast:13 row_mask:0xf bank_mask:0xf\n"
    "v_and_b32_dpp v15, v37, v33 row_newbcast:13 row_mask:0xf bank_mask:0xf\n"
    "v_smfmac_f32_16x16x64_bf16 v[28:31], v[8:11], v[212:219], v34\n"
    "buffer_load_dwordx4 v0, s[40:43], 0 offen offset:1024 nt lds\n"
    "v_and_b32_dpp v8, v37, v32 row_newbcast:14 row_mask:0xf bank_mask:0xf\n"
    "v_and_b32_dpp v9, v37, v33 row_newbcast:14 row_mask:0xf bank_mask:0xf\n"
    "v_and_b32_dpp v10, v37, v32 row_newbcast:15 row_mask:0xf bank_mask:0xf\n"
    "v_and_b32_dpp v11, v37, v33 row_newbcast:15 row_mask:0xf bank_mask:0xf\n"
    "v_smfmac_f32_16x16x64_bf16 v[28:31], v[12:15], v[220:227], v34\n"
    "v_and_b32_dpp v12, v38, v32 row_newbcast:0 row_mask:0xf bank_mask:0xf\n"
    "v_and_b32_dpp v13, v38, v33 row_newbcast:0 row_mask:0xf bank_mask:0xf\n"
    "v_and_b32_dpp v14, v38, v32 row_newbcast:1 row_mask:0xf bank_mask:0xf\n"
    "v_and_b32_dpp v15, v38, v33 row_newbcast:1 row_mask:0xf bank_mask:0xf\n"
    "v_smfmac_f32_16x16x64_bf16 v[28:31], v[8:11], v[228:235], v34\n"
    "v_and_b32_dpp v8, v38, v32 row_newbcast:2 row_mask:0xf bank_mask:0xf\n"
    "v_and_b32_dpp v9, v38, v33 row_newbcast:2 row_mask:0xf bank_mask:0xf\n"
    "v_and_b32_dpp v10, v38, v32 row_newbcast:3 row_mask:0xf bank_mask:0xf\n"
    "v_and_b32_dpp v11, v38, v33 row_newbcast:3 row_mask:0xf bank_mask:0xf\n"
    "v_smfmac_f32_16x16x64_bf16 v[28:31], v[12:15], v[236:243], v34\n"
    "s_nop 0\n"
    "v_smfmac_f32_16x16x64_bf16 v[28:31], v[8:11], v[244:251], v34\n"
    "s_cmp_eq_u32 s61, 0\n"
    "s_nop 6\n"
    "s_cbranch_scc1 Lf3x_%=\n"
    "v_mov_b32 v8, 0\n"
    "v_mov_b32 v9, 1\n"
    "v_mov_b32 v3, 0\n"
    "v_and_b32 v36, 0x1f0, v39\n"
    "s_add_u32 s69, s61, 8\n"
    "s_mul_i32 s69, s69, 0x9e3779b1\n"
    "s_add_u32 s69, s69, s92\n"
    "s_mov_b32 exec_hi, 0\n"
    "ds_write_b128 v39, v[28:31] offset:6336\n"
    "s_mov_b64 exec, 1\n"
    "ds_add_rtn_u32 v9, v8, v9 offset:8416\n"
    "s_mov_b64 exec, -1\n"
    "ds_read_b128 v[12:15], v3 offset:8384\n"
    "ds_read_b128 v[16:19], v36 offset:6336\n"
    "ds_read_b128 v[20:23], v36 offset:6848\n"
    "ds_read_b128 v[24:27], v36 offset:7360\n"
    "s_waitcnt lgkmcnt(4)\n"
    "v_readfirstlane_b32 s68, v9\n"
    "ds_read_b128 v[8:11], v3 offset:8400\n"
    "ds_read_b128 v[28:31], v36 offset:7872\n"
    "s_and_b32 s68, s68, 3\n"
    "s_cmp_eq_u32 s68, 3\n"
    "s_cbranch_scc0 Lf3w_%=\n"
    "s_add_u32 s68, s66, 258048\n"
    "s_waitcnt lgkmcnt(5)\n"
    "v_max3_f32 v37, v12, v13, v14\n"
    "v_max_f32 v37, v37, v15\n"
    "v_sub_f32 v12, v12, v37\n"
    "v_sub_f32 v13, v13, v37\n"
    "v_sub_f32 v14, v14, v37\n"
    "v_sub_f32 v15, v15, v37\n"
    "v_exp_f32 v12, v12\n"
    "v_exp_f32 v13, v13\n"
    "v_exp_f32 v14, v14\n"
    "v_exp_f32 v15, v15\n"
    "s_waitcnt lgkmcnt(1)\n"
    "v_mul_f32 v38, v12, v8\n"
    "v_fmac_f32 v38, v13, v9\n"
    "v_fmac_f32 v38, v14, v10\n"
    "v_fmac_f32 v38, v15, v11\n"
    "v_rcp_f32 v8, v38\n"
    "v_log_f32 v38, v38\n"
    "v_pk_mul_f32 v[16:17], v[12:13], v[16:17] op_sel_hi:[0,1]\n"
    "v_pk_mul_f32 v[18:19], v[12:13], v[18:19] op_sel_hi:[0,1]\n"
    "s_waitcnt lgkmcnt(0)\n"
    "v_pk_fma_f32 v[16:17], v[12:13], v[20:21], v[16:17] op_sel:[1,0,0] op_sel_hi:[1,1,1]\n"
    "v_pk_fma_f32 v[18:19], v[12:13], v[22:23], v[18:19] op_sel:[1,0,0] op_sel_hi:[1,1,1]\n"
    "v_pk_fma_f32 v[16:17], v[14:15], v[24:25], v[16:17] op_sel_hi:[0,1,1]\n"
    "v_pk_fma_f32 v[18:19], v[14:15], v[26:27], v[18:19] op_sel_hi:[0,1,1]\n"
    "v_pk_fma_f32 v[16:17], v[14:15], v[28:29], v[16:17] op_sel:[1,0,0] op_sel_hi:[1,1,1]\n"
    "v_pk_fma_f32 v[18:19], v[14:15], v[30:31], v[18:19] op_sel:[1,0,0] op_sel_hi:[1,1,1]\n"
    "v_pk_mul_f32 v[16:17], v[8:9], v[16:17] op_sel_hi:[0,1]\n"
    "v_pk_mul_f32 v[18:19], v[8:9], v[18:19] op_sel_hi:[0,1]\n"
    "v_cvt_pk_bf16_f32 v20, v16, v17\n"
    "v_cvt_pk_bf16_f32 v22, v16, v17\n"
    "v_cvt_pk_bf16_f32 v21, v18, v19\n"
    "v_cvt_pk_bf16_f32 v23, v18, v19\n"
    "v_add_f32 v38, v38, v37\n"
    "v_permlane16_swap_b32 v20, v22\n"
    "v_permlane16_swap_b32 v21, v23\n"
    "v_add3_u32 v24, v20, v22, v38\n"
    "v_add3_u32 v25, v21, v23, s69\n"
    "v_add_u32 v24, v24, v25\n"
    "v_cndmask_b32 v20, v20, v38, s[80:81]\n"
    "v_cndmask_b32 v22, v22, v38, s[80:81]\n"
    "v_mov_b32_dpp v23, v24 row_ror:8 row_mask:0xe bank_mask:0xf\n"
    "s_mov_b64 exec, 0xffffff\n"
    "v_cndmask_b32 v21, v21, v24, s[80:81]\n"
    "buffer_store_dwordx4 v[20:23], v1, s[48:51], s68 offen sc1\n"
    "s_mov_b64 exec, -1\n"
    "s_nop 0\n"
    "Lf3w_%=:\n"
    "s_waitcnt lgkmcnt(0)\n"
    "Lf3x_%=:\n"
    "s_cmp_ge_u32 s61, 8\n"
    "s_cbranch_scc1 Lend_%=\n"
    "v_add_u32 v3, s64, v1\n"
    "v_add_u32 v38, s62, v1\n"
    "ds_read_b128 v[196:199], v38 offset:0\n"
    "ds_read_b128 v[200:203], v38 offset:1024\n"
    "ds_read_b128 v[204:207], v38 offset:2048\n"
    "ds_read_b128 v[208:211], v38 offset:3072\n"
    "ds_read_b128 v[212:215], v38 offset:4096\n"
    "ds_read_b128 v[216:219], v38 offset:5120\n"
    "ds_read_b128 v[220:223], v38 offset:6144\n"
    "ds_read_b128 v[224:227], v3 offset:14336\n"
    "ds_read_b128 v[228:231], v3 offset:15360\n"
    "ds_read_b128 v[232:235], v3 offset:16384\n"
    "ds_read_b128 v[236:239], v3 offset:17408\n"
    "ds_read_b128 v[240:243], v3 offset:18432\n"
    "ds_read_b128 v[244:247], v3 offset:19456\n"
    "ds_read_b128 v[248:251], v3 offset:20480\n"
    "ds_read_b128 v[252:255], v3 offset:21504\n"
    "s_mul_i32 s97, s96, 2\n"
    "s_add_u32 s97, s97, s95\n"
    "Lg57_%=:\n"
    "s_memrealtime s[98:99]\n"
    "s_waitcnt lgkmcnt(0)\n"
    "s_lshl_b32 s98, s98, 4\n"
    "s_sub_u32 s98, s98, s97\n"
    "s_cmp_lt_i32 s98, 0\n"
    "s_cbranch_scc1 Lg57_%=\n"
    "s_waitcnt vmcnt(7)\n"
    "buffer_load_dwordx4 v0, s[40:43], 0 offen offset:2048 nt lds\n"
    "s_mul_i32 s97, s96, 3\n"
    "s_add_u32 s97, s97, s95\n"
    "Lg58_%=:\n"
    "s_memrealtime s[98:99]\n"
    "s_waitcnt lgkmcnt(0)\n"
    "s_lshl_b32 s98, s98, 4\n"
    "s_sub_u32 s98, s98, s97\n"
    "s_cmp_lt_i32 s98, 0\n"
    "s_cbranch_scc1 Lg58_%=\n"
    "s_waitcnt vmcnt(7)\n"
    "buffer_load_dwordx4 v0, s[40:43], 0 offen offset:3072 nt lds\n"
    "s_mul_i32 s97, s96, 4\n"
    "s_add_u32 s97, s97, s95\n"
    "Lg59_%=:\n"
    "s_memrealtime s[98:99]\n"
    "s_waitcnt lgkmcnt(0)\n"
    "s_lshl_b32 s98, s98, 4\n"
    "s_sub_u32 s98, s98, s97\n"
    "s_cmp_lt_i32 s98, 0\n"
    "s_cbranch_scc1 Lg59_%=\n"
    "s_waitcnt vmcnt(7)\n"
    "s_add_u32 m0, s64, 26624\n"
    "s_nop 0\n"
    "buffer_load_dwordx4 v0, s[40:43], s52 offen offset:0 nt lds\n"
    "s_waitcnt vmcnt(5)\n"
    "s_waitcnt vmcnt(7)\n"
    "Lqcc_%=:\n"
    "v_max3_u32 v3, v4, v5, v6\n"
    "v_max_u32 v3, v3, v7\n"
    "v_cmp_eq_u32 vcc, -1, v3\n"
    "s_cbranch_vccz Lqcd_%=\n"
    "s_nop 3\n"
    "Lqcr_%=:\n"
    "s_sub_u32 s67, s67, 1\n"
    "s_cmp_lt_i32 s67, 0\n"
    "s_cbranch_scc1 Lqcd_%=\n"
    "s_add_u32 s68, s61, 0\n"
    "s_lshl_b32 s68, s68, 13\n"
    "s_add_u32 s68, s68, s65\n"
    "v_lshrrev_b32 v3, 6, v1\n"
    "v_lshlrev_b32 v3, 4, v3\n"
    "buffer_load_dwordx4 v[4:7], v3, s[48:51], s68 offen sc1\n"
    "s_memrealtime s[98:99]\n"
    "s_mul_i32 s97, s96, 5\n"
    "s_waitcnt lgkmcnt(0)\n"
    "s_lshl_b32 s98, s98, 4\n"
    "s_sub_u32 s98, s98, s97\n"
    "s_sub_u32 s97, s98, s95\n"
    "s_cmp_gt_i32 s97, 0\n"
    "s_cselect_b32 s95, s98, s95\n"
    "s_waitcnt vmcnt(0)\n"
    "s_branch Lqcc_%=\n"
    "Lqcd_%=:\n"
    "v_cndmask_b32 v8, 0, v4, s[70:71]\n"
    "v_cndmask_b32 v9, 0, v5, s[70:71]\n"
    "v_cndmask_b32 v10, 0, v6, s[70:71]\n"
    "v_cndmask_b32 v11, 0, v7, s[70:71]\n"
    "v_cndmask_b32 v12, 0, v4, s[72:73]\n"
    "v_cndmask_b32 v13, 0, v5, s[72:73]\n"
    "v_cndmask_b32 v14, 0, v6, s[72:73]\n"
    "v_cndmask_b32 v15, 0, v7, s[72:73]\n"
    "v_cndmask_b32 v16, 0, v4, s[74:75]\n"
    "v_cndmask_b32 v17, 0, v5, s[74:75]\n"
    "v_cndmask_b32 v18, 0, v6, s[74:75]\n"
    "v_cndmask_b32 v19, 0, v7, s[74:75]\n"
    "v_cndmask_b32 v4, 0, v4, s[76:77]\n"
    "v_cndmask_b32 v5, 0, v5, s[76:77]\n"
    "v_cndmask_b32 v6, 0, v6, s[76:77]\n"
    "v_cndmask_b32 v7, 0, v7, s[76:77]\n"
    "v_mfma_f32_16x16x32_bf16 v[20:23], a[0:3], v[8:11], 0\n"
    "v_mfma_f32_16x16x32_bf16 v[20:23], a[4:7], v[12:15], v[20:23]\n"
    "v_mfma_f32_16x16x32_bf16 v[20:23], a[8:11], v[16:19], v[20:23]\n"
    "v_mfma_f32_16x16x32_bf16 v[20:23], a[12:15], v[4:7], v[20:23]\n"
    "buffer_load_dwordx4 v0, s[40:43], s52 offen offset:1024 nt lds\n"
    "v_mfma_f32_16x16x32_bf16 v[24:27], a[16:19], v[8:11], 0\n"
    "v_mfma_f32_16x16x32_bf16 v[24:27], a[20:23], v[12:15], v[24:27]\n"
    "v_mfma_f32_16x16x32_bf16 v[24:27], a[24:27], v[16:19], v[24:27]\n"
    "v_mfma_f32_16x16x32_bf16 v[24:27], a[28:31], v[4:7], v[24:27]\n"
    "v_mfma_f32_16x16x32_bf16 v[28:31], a[32:35], v[8:11], 0\n"
    "s_nop 1\n"
    "v_add_f32_dpp v36, v21, v20 row_ror:12 row_mask:0xf bank_mask:0x1\n"
    "v_mfma_f32_16x16x32_bf16 v[28:31], a[36:39], v[12:15], v[28:31]\n"
    "s_nop 0\n"
    "v_add_f32_dpp v36, v22, v36 row_ror:8 row_mask:0xf bank_mask:0x1\n"
    "v_mfma_f32_16x16x32_bf16 v[28:31], a[40:43], v[16:19], v[28:31]\n"
    "s_nop 0\n"
    "v_add_f32_dpp v36, v23, v36 row_ror:4 row_mask:0xf bank_mask:0x1\n"
    "v_mfma_f32_16x16x32_bf16 v[28:31], a[44:47], v[4:7], v[28:31]\n"
    "v_mfma_f32_16x16x32_bf16 v[20:23], a[48:51], v[8:11], 0\n"
    "buffer_load_dwordx4 v0, s[40:43], s52 offen offset:2048 nt lds\n"
    "v_add_f32_dpp v36, v26, v25 row_ror:12 row_mask:0xf bank_mask:0x2\n"
    "v_mfma_f32_16x16x32_bf16 v[20:23], a[52:55], v[12:15], v[20:23]\n"
    "s_nop 0\n"
    "v_add_f32_dpp v36, v27, v36 row_ror:8 row_mask:0xf bank_mask:0x2\n"
    "v_mfma_f32_16x16x32_bf16 v[20:23], a[56:59], v[16:19], v[20:23]\n"
    "s_nop 0\n"
    "v_add_f32_dpp v36, v24, v36 row_ror:4 row_mask:0xf bank_mask:0x2\n"
    "v_mfma_f32_16x16x32_bf16 v[20:23], a[60:63], v[4:7], v[20:23]\n"
    "v_mfma_f32_16x16x32_bf16 v[24:27], a[64:67], v[8:11], 0\n"
    "v_add_f32_dpp v36, v31, v30 row_ror:12 row_mask:0xf bank_mask:0x4\n"
    "v_mfma_f32_16x16x32_bf16 v[24:27], a[68:71], v[12:15], v[24:27]\n"
    "s_nop 0\n"
    "v_add_f32_dpp v36, v28, v36 row_ror:8 row_mask:0xf bank_mask:0x4\n"
    "v_mfma_f32_16x16x32_bf16 v[24:27], a[72:75], v[16:19], v[24:27]\n"
    "s_nop 0\n"
    "v_add_f32_dpp v36, v29, v36 row_ror:4 row_mask:0xf bank_mask:0x4\n"
    "v_mfma_f32_16x16x32_bf16 v[24:27], a[76:79], v[4:7], v[24:27]\n"
    "v_mfma_f32_16x16x32_bf16 v[28:31], a[80:83], v[8:11], 0\n"
    "v_add_f32_dpp v36, v20, v23 row_ror:12 row_mask:0xf bank_mask:0x8\n"
    "v_mfma_f32_16x16x32_bf16 v[28:31], a[84:87], v[12:15], v[28:31]\n"
    "buffer_load_dwordx4 v0, s[40:43], s52 offen offset:3072 nt lds\n"
    "v_add_f32_dpp v36, v21, v36 row_ror:8 row_mask:0xf bank_mask:0x8\n"
    "v_mfma_f32_16x16x32_bf16 v[28:31], a[88:91], v[16:19], v[28:31]\n"
    "s_nop 0\n"
    "v_add_f32_dpp v36, v22, v36 row_ror:4 row_mask:0xf bank_mask:0x8\n"
    "v_mfma_f32_16x16x32_bf16 v[28:31], a[92:95], v[4:7], v[28:31]\n"
    "v_mfma_f32_16x16x32_bf16 v[20:23], a[96:99], v[8:11], 0\n"
    "v_add_f32_dpp v37, v25, v24 row_ror:12 row_mask:0xf bank_mask:0x1\n"
    "v_mfma_f32_16x16x32_bf16 v[20:23], a[100:103], v[12:15], v[20:23]\n"
    "s_nop 0\n"
    "v_add_f32_dpp v37, v26, v37 row_ror:8 row_mask:0xf bank_mask:0x1\n"
    "v_mfma_f32_16x16x32_bf16 v[20:23], a[104:107], v[16:19], v[20:23]\n"
    "s_nop 0\n"
    "v_add_f32_dpp v37, v27, v37 row_ror:4 row_mask:0xf bank_mask:0x1\n"
    "v_mfma_f32_16x16x32_bf16 v[20:23], a[108:111], v[4:7], v[20:23]\n"
    "v_mfma_f32_16x16x32_bf16 v[24:27], a[112:115], v[8:11], 0\n"
    "v_add_f32_dpp v37, v30, v29 row_ror:12 row_mask:0xf bank_mask:0x2\n"
    "v_mfma_f32_16x16x32_bf16 v[24:27], a[116:119], v[12:15], v[24:27]\n"
    "s_nop 0\n"
    "v_add_f32_dpp v37, v31, v37 row_ror:8 row_mask:0xf bank_mask:0x2\n"
    "v_mfma_f32_16x16x32_bf16 v[24:27], a[120:123], v[16:19], v[24:27]\n"
    "s_add_u32 m0, s64, 30720\n"
    "s_nop 0\n"
    "buffer_load_dwordx4 v0, s[40:43], s53 offen offset:0 nt lds\n"
    "v_add_f32_dpp v37, v28, v37 row_ror:4 row_mask:0xf bank_mask:0x2\n"
    "v_mfma_f32_16x16x32_bf16 v[24:27], a[124:127], v[4:7], v[24:27]\n"
    "v_mfma_f32_16x16x32_bf16 v[28:31], a[128:131], v[8:11], 0\n"
    "v_add_f32_dpp v37, v23, v22 row_ror:12 row_mask:0xf bank_mask:0x4\n"
    "v_mfma_f32_16x16x32_bf16 v[28:31], a[132:135], v[12:15], v[28:31]\n"
    "s_nop 0\n"
    "v_add_f32_dpp v37, v20, v37 row_ror:8 row_mask:0xf bank_mask:0x4\n"
    "v_mfma_f32_16x16x32_bf16 v[28:31], a[136:139], v[16:19], v[28:31]\n"
    "s_nop 0\n"
    "v_add_f32_dpp v37, v21, v37 row_ror:4 row_mask:0xf bank_mask:0x4\n"
    "v_mfma_f32_16x16x32_bf16 v[28:31], a[140:143], v[4:7], v[28:31]\n"
    "v_mfma_f32_16x16x32_bf16 v[20:23], a[144:147], v[8:11], 0\n"
    "v_add_f32_dpp v37, v24, v27 row_ror:12 row_mask:0xf bank_mask:0x8\n"
    "s_nop 1\n"
    "v_add_f32_dpp v37, v25, v37 row_ror:8 row_mask:0xf bank_mask:0x8\n"
    "s_nop 1\n"
    "v_add_f32_dpp v37, v26, v37 row_ror:4 row_mask:0xf bank_mask:0x8\n"
    "v_add_f32_dpp v38, v29, v28 row_ror:12 row_mask:0xf bank_mask:0x1\n"
    "s_nop 1\n"
    "v_add_f32_dpp v38, v30, v38 row_ror:8 row_mask:0xf bank_mask:0x1\n"
    "s_nop 1\n"
    "v_add_f32_dpp v38, v31, v38 row_ror:4 row_mask:0xf bank_mask:0x1\n"
    "s_nop 1\n"
    "v_add_f32_dpp v38, v22, v21 row_ror:12 row_mask:0xf bank_mask:0x2\n"
    "s_nop 1\n"
    "v_add_f32_dpp v38, v23, v38 row_ror:8 row_mask:0xf bank_mask:0x2\n"
    "s_nop 1\n"
    "v_add_f32_dpp v38, v20, v38 row_ror:4 row_mask:0xf bank_mask:0x2\n"
    "s_waitcnt lgkmcnt(0)\n"
    "v_cvt_pk_bf16_f32 v8, v36, v37\n"
    "v_cvt_pk_bf16_f32 v9, v38, v38\n"
    "v_lshlrev_b32 v36, 16, v8\n"
    "v_and_b32 v37, s87, v8\n"
    "v_and_b32 v38, s87, v9\n"
    "v_mul_f32 v36, s84, v36\n"
    "v_mul_f32 v37, s84, v37\n"
    "v_mul_f32 v38, s84, v38\n"
    "v_cvt_pk_bf16_f32 v8, v36, v37\n"
    "v_cvt_pk_bf16_f32 v9, v38, v38\n"
    "v_mov_b32 v10, s86\n"
    "v_lshlrev_b32 v36, 16, v8\n"
    "v_and_b32 v37, s87, v8\n"
    "v_and_b32 v38, s87, v9\n"
    "v_cndmask_b32 v38, v10, v38, s[78:79]\n"
    "v_max3_f32 v8, v36, v37, v38\n"
    "buffer_load_dwordx4 v0, s[40:43], s53 offen offset:1024 nt lds\n"
    "s_nop 0\n"
    "v_max_f32_dpp v8, v8, v8 row_ror:8 row_mask:0xf bank_mask:0xf\n"
    "s_nop 1\n"
    "v_max_f32_dpp v8, v8, v8 row_ror:4 row_mask:0xf bank_mask:0xf\n"
    "s_nop 1\n"
    "v_max_f32_dpp v8, v8, v8 row_ror:2 row_mask:0xf bank_mask:0xf\n"
    "s_nop 1\n"
    "v_max_f32_dpp v9, v8, v8 row_ror:1 row_mask:0xf bank_mask:0xf\n"
    "v_max_f32_dpp v8, v8, v8 row_ror:1 row_mask:0xf bank_mask:0xf\n"
    "s_nop 1\n"
    "v_permlane16_swap_b32 v8, v9\n"
    "v_max_f32 v10, v8, v9\n"
    "v_max_f32 v8, v8, v9\n"
    "s_nop 1\n"
    "v_permlane32_swap_b32 v8, v10\n"
    "v_max_f32 v8, v8, v10\n"
    "v_mul_f32 v16, s85, v8\n"
    "v_fma_f32 v38, v38, s85, -v16\n"
    "v_fma_f32 v36, v36, s85, -v16\n"
    "v_fma_f32 v37, v37, s85, -v16\n"
    "v_exp_f32 v38, v38\n"
    "v_exp_f32 v36, v36\n"
    "v_exp_f32 v37, v37\n"
    "v_add_f32 v17, v38, v36\n"
    "v_cvt_pk_bf16_f32 v38, v38, v38\n"
    "v_add_f32 v17, v17, v37\n"
    "v_cvt_pk_bf16_f32 v36, v36, v36\n"
    "v_cvt_pk_bf16_f32 v37, v37, v37\n"
    "v_and_b32_dpp v19, v38, v35 row_newbcast:4 row_mask:0xf bank_mask:0xf\n"
    "s_mov_b32 vcc_lo, 0x3030303\n"
    "s_mov_b32 vcc_hi, 0x3030303\n"
    "v_cndmask_b32 v8, 0, v19, vcc\n"
    "s_mov_b32 vcc_lo, 0xc0c0c0c\n"
    "s_mov_b32 vcc_hi, 0xc0c0c0c\n"
    "v_cndmask_b32 v9, 0, v19, vcc\n"
    "s_mov_b32 vcc_lo, 0x30303030\n"
    "s_mov_b32 vcc_hi, 0x30303030\n"
    "v_cndmask_b32 v10, 0, v19, vcc\n"
    "s_mov_b32 vcc_lo, 0xc0c0c0c0\n"
    "s_mov_b32 vcc_hi, 0xc0c0c0c0\n"
    "v_cndmask_b32 v11, 0, v19, vcc\n"
    "v_and_b32_dpp v12, v36, v32 row_newbcast:0 row_mask:0xf bank_mask:0xf\n"
    "v_and_b32_dpp v13, v36, v33 row_newbcast:0 row_mask:0xf bank_mask:0xf\n"
    "v_and_b32_dpp v14, v36, v32 row_newbcast:1 row_mask:0xf bank_mask:0xf\n"
    "v_and_b32_dpp v15, v36, v33 row_newbcast:1 row_mask:0xf bank_mask:0xf\n"
    "v_mfma_f32_16x16x32_bf16 v[28:31], v[8:11], v[196:199], 0\n"
    "v_and_b32_dpp v8, v36, v32 row_newbcast:2 row_mask:0xf bank_mask:0xf\n"
    "v_and_b32_dpp v9, v36, v33 row_newbcast:2 row_mask:0xf bank_mask:0xf\n"
    "v_and_b32_dpp v10, v36, v32 row_newbcast:3 row_mask:0xf bank_mask:0xf\n"
    "v_and_b32_dpp v11, v36, v33 row_newbcast:3 row_mask:0xf bank_mask:0xf\n"
    "v_smfmac_f32_16x16x64_bf16 v[28:31], v[12:15], v[200:207], v34\n"
    "v_add_f32_dpp v17, v17, v17 row_ror:8 row_mask:0xf bank_mask:0xf\n"
    "v_and_b32_dpp v12, v36, v32 row_newbcast:4 row_mask:0xf bank_mask:0xf\n"
    "v_and_b32_dpp v13, v36, v33 row_newbcast:4 row_mask:0xf bank_mask:0xf\n"
    "v_and_b32_dpp v14, v36, v32 row_newbcast:5 row_mask:0xf bank_mask:0xf\n"
    "v_and_b32_dpp v15, v36, v33 row_newbcast:5 row_mask:0xf bank_mask:0xf\n"
    "v_smfmac_f32_16x16x64_bf16 v[28:31], v[8:11], v[208:215], v34\n"
    "v_add_f32_dpp v17, v17, v17 row_ror:4 row_mask:0xf bank_mask:0xf\n"
    "buffer_load_dwordx4 v0, s[40:43], s53 offen offset:2048 nt lds\n"
    "v_and_b32_dpp v8, v36, v32 row_newbcast:6 row_mask:0xf bank_mask:0xf\n"
    "v_and_b32_dpp v9, v36, v33 row_newbcast:6 row_mask:0xf bank_mask:0xf\n"
    "v_and_b32_dpp v10, v36, v32 row_newbcast:7 row_mask:0xf bank_mask:0xf\n"
    "v_and_b32_dpp v11, v36, v33 row_newbcast:7 row_mask:0xf bank_mask:0xf\n"
    "v_smfmac_f32_16x16x64_bf16 v[28:31], v[12:15], v[216:223], v34\n"
    "v_add_f32_dpp v17, v17, v17 row_ror:2 row_mask:0xf bank_mask:0xf\n"
    "v_and_b32_dpp v12, v36, v32 row_newbcast:8 row_mask:0xf bank_mask:0xf\n"
    "v_and_b32_dpp v13, v36, v33 row_newbcast:8 row_mask:0xf bank_mask:0xf\n"
    "v_and_b32_dpp v14, v36, v32 row_newbcast:9 row_mask:0xf bank_mask:0xf\n"
    "v_and_b32_dpp v15, v36, v33 row_newbcast:9 row_mask:0xf bank_mask:0xf\n"
    "v_smfmac_f32_16x16x64_bf16 v[28:31], v[8:11], v[224:231], v34\n"
    "v_add_f32_dpp v17, v17, v17 row_ror:1 row_mask:0xf bank_mask:0xf\n"
    "v_and_b32_dpp v8, v36, v32 row_newbcast:10 row_mask:0xf bank_mask:0xf\n"
    "v_and_b32_dpp v9, v36, v33 row_newbcast:10 row_mask:0xf bank_mask:0xf\n"
    "v_and_b32_dpp v10, v36, v32 row_newbcast:11 row_mask:0xf bank_mask:0xf\n"
    "v_and_b32_dpp v11, v36, v33 row_newbcast:11 row_mask:0xf bank_mask:0xf\n"
    "v_smfmac_f32_16x16x64_bf16 v[28:31], v[12:15], v[232:239], v34\n"
    "v_mov_b32 v18, v17\n"
    "v_and_b32_dpp v12, v36, v32 row_newbcast:12 row_mask:0xf bank_mask:0xf\n"
    "v_and_b32_dpp v13, v36, v33 row_newbcast:12 row_mask:0xf bank_mask:0xf\n"
    "v_and_b32_dpp v14, v36, v32 row_newbcast:13 row_mask:0xf bank_mask:0xf\n"
    "v_and_b32_dpp v15, v36, v33 row_newbcast:13 row_mask:0xf bank_mask:0xf\n"
    "v_smfmac_f32_16x16x64_bf16 v[28:31], v[8:11], v[240:247], v34\n"
    "v_permlane16_swap_b32 v17, v18\n"
    "v_add_f32 v17, v17, v18\n"
    "v_and_b32_dpp v8, v36, v32 row_newbcast:14 row_mask:0xf bank_mask:0xf\n"
    "v_and_b32_dpp v9, v36, v33 row_newbcast:14 row_mask:0xf bank_mask:0xf\n"
    "v_and_b32_dpp v10, v36, v32 row_newbcast:15 row_mask:0xf bank_mask:0xf\n"
    "v_and_b32_dpp v11, v36, v33 row_newbcast:15 row_mask:0xf bank_mask:0xf\n"
    "v_smfmac_f32_16x16x64_bf16 v[28:31], v[12:15], v[248:255], v34\n"
    "v_mov_b32 v18, v17\n"
    "v_and_b32_dpp v12, v37, v32 row_newbcast:0 row_mask:0xf bank_mask:0xf\n"
    "v_and_b32_dpp v13, v37, v33 row_newbcast:0 row_mask:0xf bank_mask:0xf\n"
    "v_and_b32_dpp v14, v37, v32 row_newbcast:1 row_mask:0xf bank_mask:0xf\n"
    "v_and_b32_dpp v15, v37, v33 row_newbcast:1 row_mask:0xf bank_mask:0xf\n"
    "v_smfmac_f32_16x16x64_bf16 v[28:31], v[8:11], a[148:155], v34\n"
    "v_permlane32_swap_b32 v17, v18\n"
    "v_add_f32 v17, v17, v18\n"
    "buffer_load_dwordx4 v0, s[40:43], s53 offen offset:3072 nt lds\n"
    "v_and_b32_dpp v8, v37, v32 row_newbcast:2 row_mask:0xf bank_mask:0xf\n"
    "v_and_b32_dpp v9, v37, v33 row_newbcast:2 row_mask:0xf bank_mask:0xf\n"
    "v_and_b32_dpp v10, v37, v32 row_newbcast:3 row_mask:0xf bank_mask:0xf\n"
    "v_and_b32_dpp v11, v37, v33 row_newbcast:3 row_mask:0xf bank_mask:0xf\n"
    "v_smfmac_f32_16x16x64_bf16 v[28:31], v[12:15], a[156:163], v34\n"
    "v_mov_b32 v18, s82\n"
    "s_mov_b64 exec, 1\n"
    "ds_write_b32 v18, v16 offset:2048\n"
    "ds_write_b32 v18, v17 offset:2064\n"
    "s_mov_b64 exec, -1\n"
    "v_and_b32_dpp v12, v37, v32 row_newbcast:4 row_mask:0xf bank_mask:0xf\n"
    "v_and_b32_dpp v13, v37, v33 row_newbcast:4 row_mask:0xf bank_mask:0xf\n"
    "v_and_b32_dpp v14, v37, v32 row_newbcast:5 row_mask:0xf bank_mask:0xf\n"
    "v_and_b32_dpp v15, v37, v33 row_newbcast:5 row_mask:0xf bank_mask:0xf\n"
    "v_smfmac_f32_16x16x64_bf16 v[28:31], v[8:11], a[164:171], v34\n"
    "v_and_b32_dpp v8, v37, v32 row_newbcast:6 row_mask:0xf bank_mask:0xf\n"
    "v_and_b32_dpp v9, v37, v33 row_newbcast:6 row_mask:0xf bank_mask:0xf\n"
    "v_and_b32_dpp v10, v37, v32 row_newbcast:7 row_mask:0xf bank_mask:0xf\n"
    "v_and_b32_dpp v11, v37, v33 row_newbcast:7 row_mask:0xf bank_mask:0xf\n"
    "v_smfmac_f32_16x16x64_bf16 v[28:31], v[12:15], a[172:179], v34\n"
    "v_and_b32_dpp v12, v37, v32 row_newbcast:8 row_mask:0xf bank_mask:0xf\n"
    "v_and_b32_dpp v13, v37, v33 row_newbcast:8 row_mask:0xf bank_mask:0xf\n"
    "v_and_b32_dpp v14, v37, v32 row_newbcast:9 row_mask:0xf bank_mask:0xf\n"
    "v_and_b32_dpp v15, v37, v33 row_newbcast:9 row_mask:0xf bank_mask:0xf\n"
    "v_smfmac_f32_16x16x64_bf16 v[28:31], v[8:11], a[180:187], v34\n"
    "v_and_b32_dpp v8, v37, v32 row_newbcast:10 row_mask:0xf bank_mask:0xf\n"
    "v_and_b32_dpp v9, v37, v33 row_newbcast:10 row_mask:0xf bank_mask:0xf\n"
    "v_and_b32_dpp v10, v37, v32 row_newbcast:11 row_mask:0xf bank_mask:0xf\n"
    "v_and_b32_dpp v11, v37, v33 row_newbcast:11 row_mask:0xf bank_mask:0xf\n"
    "v_smfmac_f32_16x16x64_bf16 v[28:31], v[12:15], a[188:195], v34\n"
    "v_and_b32_dpp v12, v37, v32 row_newbcast:12 row_mask:0xf bank_mask:0xf\n"
    "v_and_b32_dpp v13, v37, v33 row_newbcast:12 row_mask:0xf bank_mask:0xf\n"
    "v_and_b32_dpp v14, v37, v32 row_newbcast:13 row_mask:0xf bank_mask:0xf\n"
    "v_and_b32_dpp v15, v37, v33 row_newbcast:13 row_mask:0xf bank_mask:0xf\n"
    "v_smfmac_f32_16x16x64_bf16 v[28:31], v[8:11], a[196:203], v34\n"
    "s_add_u32 m0, s64, 34816\n"
    "s_nop 0\n"
    "buffer_load_dwordx4 v0, s[40:43], s54 offen offset:0 nt lds\n"
    "v_and_b32_dpp v8, v37, v32 row_newbcast:14 row_mask:0xf bank_mask:0xf\n"
    "v_and_b32_dpp v9, v37, v33 row_newbcast:14 row_mask:0xf bank_mask:0xf\n"
    "v_and_b32_dpp v10, v37, v32 row_newbcast:15 row_mask:0xf bank_mask:0xf\n"
    "v_and_b32_dpp v11, v37, v33 row_newbcast:15 row_mask:0xf bank_mask:0xf\n"
    "v_smfmac_f32_16x16x64_bf16 v[28:31], v[12:15], a[204:211], v34\n"
    "v_and_b32_dpp v12, v38, v32 row_newbcast:0 row_mask:0xf bank_mask:0xf\n"
    "v_and_b32_dpp v13, v38, v33 row_newbcast:0 row_mask:0xf bank_mask:0xf\n"
    "v_and_b32_dpp v14, v38, v32 row_newbcast:1 row_mask:0xf bank_mask:0xf\n"
    "v_and_b32_dpp v15, v38, v33 row_newbcast:1 row_mask:0xf bank_mask:0xf\n"
    "v_smfmac_f32_16x16x64_bf16 v[28:31], v[8:11], a[212:219], v34\n"
    "v_and_b32_dpp v8, v38, v32 row_newbcast:2 row_mask:0xf bank_mask:0xf\n"
    "v_and_b32_dpp v9, v38, v33 row_newbcast:2 row_mask:0xf bank_mask:0xf\n"
    "v_and_b32_dpp v10, v38, v32 row_newbcast:3 row_mask:0xf bank_mask:0xf\n"
    "v_and_b32_dpp v11, v38, v33 row_newbcast:3 row_mask:0xf bank_mask:0xf\n"
    "v_smfmac_f32_16x16x64_bf16 v[28:31], v[12:15], a[220:227], v34\n"
    "s_nop 0\n"
    "v_smfmac_f32_16x16x64_bf16 v[28:31], v[8:11], a[228:235], v34\n"
    "v_mov_b32 v8, 0\n"
    "v_mov_b32 v9, 1\n"
    "v_mov_b32 v3, 0\n"
    "v_and_b32 v36, 0x1f0, v39\n"
    "s_add_u32 s69, s61, 1\n"
    "s_mul_i32 s69, s69, 0x9e3779b1\n"
    "s_add_u32 s69, s69, s92\n"
    "s_mov_b32 exec_hi, 0\n"
    "ds_write_b128 v39, v[28:31] offset:0\n"
    "s_mov_b64 exec, 1\n"
    "ds_add_rtn_u32 v9, v8, v9 offset:2080\n"
    "s_mov_b64 exec, -1\n"
    "ds_read_b128 v[12:15], v3 offset:2048\n"
    "ds_read_b128 v[16:19], v36 offset:0\n"
    "ds_read_b128 v[20:23], v36 offset:512\n"
    "ds_read_b128 v[24:27], v36 offset:1024\n"
    "s_waitcnt lgkmcnt(4)\n"
    "v_readfirstlane_b32 s68, v9\n"
    "ds_read_b128 v[8:11], v3 offset:2064\n"
    "ds_read_b128 v[28:31], v36 offset:1536\n"
    "s_and_b32 s68, s68, 3\n"
    "s_cmp_eq_u32 s68, 3\n"
    "s_cbranch_scc0 Lf4w_%=\n"
    "s_add_u32 s68, s66, 0\n"
    "s_waitcnt lgkmcnt(5)\n"
    "v_max3_f32 v37, v12, v13, v14\n"
    "v_max_f32 v37, v37, v15\n"
    "v_sub_f32 v12, v12, v37\n"
    "v_sub_f32 v13, v13, v37\n"
    "v_sub_f32 v14, v14, v37\n"
    "v_sub_f32 v15, v15, v37\n"
    "v_exp_f32 v12, v12\n"
    "v_exp_f32 v13, v13\n"
    "v_exp_f32 v14, v14\n"
    "v_exp_f32 v15, v15\n"
    "s_waitcnt lgkmcnt(1)\n"
    "v_mul_f32 v38, v12, v8\n"
    "v_fmac_f32 v38, v13, v9\n"
    "v_fmac_f32 v38, v14, v10\n"
    "v_fmac_f32 v38, v15, v11\n"
    "v_rcp_f32 v8, v38\n"
    "v_log_f32 v38, v38\n"
    "v_pk_mul_f32 v[16:17], v[12:13], v[16:17] op_sel_hi:[0,1]\n"
    "v_pk_mul_f32 v[18:19], v[12:13], v[18:19] op_sel_hi:[0,1]\n"
    "s_waitcnt lgkmcnt(0)\n"
    "v_pk_fma_f32 v[16:17], v[12:13], v[20:21], v[16:17] op_sel:[1,0,0] op_sel_hi:[1,1,1]\n"
    "v_pk_fma_f32 v[18:19], v[12:13], v[22:23], v[18:19] op_sel:[1,0,0] op_sel_hi:[1,1,1]\n"
    "v_pk_fma_f32 v[16:17], v[14:15], v[24:25], v[16:17] op_sel_hi:[0,1,1]\n"
    "v_pk_fma_f32 v[18:19], v[14:15], v[26:27], v[18:19] op_sel_hi:[0,1,1]\n"
    "v_pk_fma_f32 v[16:17], v[14:15], v[28:29], v[16:17] op_sel:[1,0,0] op_sel_hi:[1,1,1]\n"
    "v_pk_fma_f32 v[18:19], v[14:15], v[30:31], v[18:19] op_sel:[1,0,0] op_sel_hi:[1,1,1]\n"
    "v_pk_mul_f32 v[16:17], v[8:9], v[16:17] op_sel_hi:[0,1]\n"
    "v_pk_mul_f32 v[18:19], v[8:9], v[18:19] op_sel_hi:[0,1]\n"
    "v_cvt_pk_bf16_f32 v20, v16, v17\n"
    "v_cvt_pk_bf16_f32 v22, v16, v17\n"
    "v_cvt_pk_bf16_f32 v21, v18, v19\n"
    "v_cvt_pk_bf16_f32 v23, v18, v19\n"
    "v_add_f32 v38, v38, v37\n"
    "v_permlane16_swap_b32 v20, v22\n"
    "v_permlane16_swap_b32 v21, v23\n"
    "v_add3_u32 v24, v20, v22, v38\n"
    "v_add3_u32 v25, v21, v23, s69\n"
    "v_add_u32 v24, v24, v25\n"
    "v_cndmask_b32 v20, v20, v38, s[80:81]\n"
    "v_cndmask_b32 v22, v22, v38, s[80:81]\n"
    "v_mov_b32_dpp v23, v24 row_ror:8 row_mask:0xe bank_mask:0xf\n"
    "s_mov_b64 exec, 0xffffff\n"
    "v_cndmask_b32 v21, v21, v24, s[80:81]\n"
    "buffer_store_dwordx4 v[20:23], v1, s[48:51], s68 offen sc1\n"
    "s_mov_b64 exec, -1\n"
    "s_nop 0\n"
    "Lf4w_%=:\n"
    "s_waitcnt lgkmcnt(0)\n"
    "Lf4x_%=:\n"
    "s_mul_i32 s97, s96, 13\n"
    "s_add_u32 s97, s97, s95\n"
    "Lg60_%=:\n"
    "s_memrealtime s[98:99]\n"
    "s_waitcnt lgkmcnt(0)\n"
    "s_lshl_b32 s98, s98, 4\n"
    "s_sub_u32 s98, s98, s97\n"
    "s_cmp_lt_i32 s98, 0\n"
    "s_cbranch_scc1 Lg60_%=\n"
    "s_waitcnt vmcnt(7)\n"
    "buffer_load_dwordx4 v0, s[40:43], s54 offen offset:1024 nt lds\n"
    "s_mul_i32 s97, s96, 14\n"
    "s_add_u32 s97, s97, s95\n"
    "Lg61_%=:\n"
    "s_memrealtime s[98:99]\n"
    "s_waitcnt lgkmcnt(0)\n"
    "s_lshl_b32 s98, s98, 4\n"
    "s_sub_u32 s98, s98, s97\n"
    "s_cmp_lt_i32 s98, 0\n"
    "s_cbranch_scc1 Lg61_%=\n"
    "s_waitcnt vmcnt(7)\n"
    "buffer_load_dwordx4 v0, s[40:43], s54 offen offset:2048 nt lds\n"
    "s_mul_i32 s97, s96, 15\n"
    "s_add_u32 s97, s97, s95\n"
    "Lg62_%=:\n"
    "s_memrealtime s[98:99]\n"
    "s_waitcnt lgkmcnt(0)\n"
    "s_lshl_b32 s98, s98, 4\n"
    "s_sub_u32 s98, s98, s97\n"
    "s_cmp_lt_i32 s98, 0\n"
    "s_cbranch_scc1 Lg62_%=\n"
    "s_waitcnt vmcnt(7)\n"
    "buffer_load_dwordx4 a[236:239], v0, s[40:43], s54 offen offset:3072 nt\n"
    "s_mul_i32 s97, s96, 16\n"
    "s_add_u32 s97, s97, s95\n"
    "Lg63_%=:\n"
    "s_memrealtime s[98:99]\n"
    "s_waitcnt lgkmcnt(0)\n"
    "s_lshl_b32 s98, s98, 4\n"
    "s_sub_u32 s98, s98, s97\n"
    "s_cmp_lt_i32 s98, 0\n"
    "s_cbranch_scc1 Lg63_%=\n"
    "s_waitcnt vmcnt(7)\n"
    "buffer_load_dwordx4 a[240:243], v0, s[40:43], s55 offen offset:0 nt\n"
    "s_mul_i32 s97, s96, 17\n"
    "s_add_u32 s97, s97, s95\n"
    "Lg64_%=:\n"
    "s_memrealtime s[98:99]\n"
    "s_waitcnt lgkmcnt(0)\n"
    "s_lshl_b32 s98, s98, 4\n"
    "s_sub_u32 s98, s98, s97\n"
    "s_cmp_lt_i32 s98, 0\n"
    "s_cbranch_scc1 Lg64_%=\n"
    "s_waitcnt vmcnt(7)\n"
    "buffer_load_dwordx4 a[244:247], v0, s[40:43], s55 offen offset:1024 nt\n"
    "s_mul_i32 s97, s96, 18\n"
    "s_add_u32 s97, s97, s95\n"
    "Lg65_%=:\n"
    "s_memrealtime s[98:99]\n"
    "s_waitcnt lgkmcnt(0)\n"
    "s_lshl_b32 s98, s98, 4\n"
    "s_sub_u32 s98, s98, s97\n"
    "s_cmp_lt_i32 s98, 0\n"
    "s_cbranch_scc1 Lg65_%=\n"
    "s_waitcnt vmcnt(7)\n"
    "buffer_load_dwordx4 a[248:251], v0, s[40:43], s55 offen offset:2048 nt\n"
    "s_mul_i32 s97, s96, 19\n"
    "s_add_u32 s97, s97, s95\n"
    "Lg66_%=:\n"
    "s_memrealtime s[98:99]\n"
    "s_waitcnt lgkmcnt(0)\n"
    "s_lshl_b32 s98, s98, 4\n"
    "s_sub_u32 s98, s98, s97\n"
    "s_cmp_lt_i32 s98, 0\n"
    "s_cbranch_scc1 Lg66_%=\n"
    "s_waitcnt vmcnt(7)\n"
    "buffer_load_dwordx4 a[252:255], v0, s[40:43], s55 offen offset:3072 nt\n"
    "s_mul_i32 s97, s96, 20\n"
    "s_add_u32 s97, s97, s95\n"
    "Lg67_%=:\n"
    "s_memrealtime s[98:99]\n"
    "s_waitcnt lgkmcnt(0)\n"
    "s_lshl_b32 s98, s98, 4\n"
    "s_sub_u32 s98, s98, s97\n"
    "s_cmp_lt_i32 s98, 0\n"
    "s_cbranch_scc1 Lg67_%=\n"
    "s_waitcnt vmcnt(7)\n"
    "buffer_load_dwordx4 v[40:43], v0, s[40:43], s56 offen offset:0 nt\n"
    "s_mul_i32 s97, s96, 21\n"
    "s_add_u32 s97, s97, s95\n"
    "Lg68_%=:\n"
    "s_memrealtime s[98:99]\n"
    "s_waitcnt lgkmcnt(0)\n"
    "s_lshl_b32 s98, s98, 4\n"
    "s_sub_u32 s98, s98, s97\n"
    "s_cmp_lt_i32 s98, 0\n"
    "s_cbranch_scc1 Lg68_%=\n"
    "s_waitcnt vmcnt(7)\n"
    "buffer_load_dwordx4 v[44:47], v0, s[40:43], s56 offen offset:1024 nt\n"
    "s_mul_i32 s97, s96, 22\n"
    "s_add_u32 s97, s97, s95\n"
    "Lg69_%=:\n"
    "s_memrealtime s[98:99]\n"
    "s_waitcnt lgkmcnt(0)\n"
    "s_lshl_b32 s98, s98, 4\n"
    "s_sub_u32 s98, s98, s97\n"
    "s_cmp_lt_i32 s98, 0\n"
    "s_cbranch_scc1 Lg69_%=\n"
    "s_waitcnt vmcnt(7)\n"
    "buffer_load_dwordx4 v[48:51], v0, s[40:43], s56 offen offset:2048 nt\n"
    "s_mul_i32 s97, s96, 23\n"
    "s_add_u32 s97, s97, s95\n"
    "Lg70_%=:\n"
    "s_memrealtime s[98:99]\n"
    "s_waitcnt lgkmcnt(0)\n"
    "s_lshl_b32 s98, s98, 4\n"
    "s_sub_u32 s98, s98, s97\n"
    "s_cmp_lt_i32 s98, 0\n"
    "s_cbranch_scc1 Lg70_%=\n"
    "s_waitcnt vmcnt(7)\n"
    "buffer_load_dwordx4 v[52:55], v0, s[40:43], s56 offen offset:3072 nt\n"
    "s_mul_i32 s97, s96, 24\n"
    "s_add_u32 s97, s97, s95\n"
    "Lg71_%=:\n"
    "s_memrealtime s[98:99]\n"
    "s_waitcnt lgkmcnt(0)\n"
    "s_lshl_b32 s98, s98, 4\n"
    "s_sub_u32 s98, s98, s97\n"
    "s_cmp_lt_i32 s98, 0\n"
    "s_cbranch_scc1 Lg71_%=\n"
    "s_waitcnt vmcnt(7)\n"
    "buffer_load_dwordx4 v[56:59], v0, s[40:43], s57 offen offset:0 nt\n"
    "s_mul_i32 s97, s96, 25\n"
    "s_add_u32 s97, s97, s95\n"
    "Lg72_%=:\n"
    "s_memrealtime s[98:99]\n"
    "s_waitcnt lgkmcnt(0)\n"
    "s_lshl_b32 s98, s98, 4\n"
    "s_sub_u32 s98, s98, s97\n"
    "s_cmp_lt_i32 s98, 0\n"
    "s_cbranch_scc1 Lg72_%=\n"
    "s_waitcnt vmcnt(7)\n"
    "buffer_load_dwordx4 v[60:63], v0, s[40:43], s57 offen offset:1024 nt\n"
    "s_mul_i32 s97, s96, 26\n"
    "s_add_u32 s97, s97, s95\n"
    "Lg73_%=:\n"
    "s_memrealtime s[98:99]\n"
    "s_waitcnt lgkmcnt(0)\n"
    "s_lshl_b32 s98, s98, 4\n"
    "s_sub_u32 s98, s98, s97\n"
    "s_cmp_lt_i32 s98, 0\n"
    "s_cbranch_scc1 Lg73_%=\n"
    "s_waitcnt vmcnt(7)\n"
    "buffer_load_dwordx4 v[64:67], v0, s[40:43], s57 offen offset:2048 nt\n"
    "s_waitcnt vmcnt(12)\n"
    "v_add_u32 v38, s64, v1\n"
    "ds_read_b128 v[192:195], v38 offset:22528\n"
    "ds_read_b128 v[196:199], v38 offset:23552\n"
    "ds_read_b128 v[200:203], v38 offset:24576\n"
    "s_mul_i32 s97, s96, 27\n"
    "s_add_u32 s97, s97, s95\n"
    "Lg74_%=:\n"
    "s_memrealtime s[98:99]\n"
    "s_waitcnt lgkmcnt(0)\n"
    "s_lshl_b32 s98, s98, 4\n"
    "s_sub_u32 s98, s98, s97\n"
    "s_cmp_lt_i32 s98, 0\n"
    "s_cbranch_scc1 Lg74_%=\n"
    "s_waitcnt vmcnt(7)\n"
    "buffer_load_dwordx4 v[68:71], v0, s[40:43], s57 offen offset:3072 nt\n"
    "ds_read_b128 v[204:207], v38 offset:25600\n"
    "ds_read_b128 v[208:211], v38 offset:26624\n"
    "ds_read_b128 v[212:215], v38 offset:27648\n"
    "s_mul_i32 s97, s96, 28\n"
    "s_add_u32 s97, s97, s95\n"
    "Lg75_%=:\n"
    "s_memrealtime s[98:99]\n"
    "s_waitcnt lgkmcnt(0)\n"
    "s_lshl_b32 s98, s98, 4\n"
    "s_sub_u32 s98, s98, s97\n"
    "s_cmp_lt_i32 s98, 0\n"
    "s_cbranch_scc1 Lg75_%=\n"
    "s_waitcnt vmcnt(7)\n"
    "buffer_load_dwordx4 v[72:75], v0, s[40:43], s58 offen offset:0 nt\n"
    "s_lshl_b32 s68, s61, 7\n"
    "s_add_u32 s68, s68, 970880\n"
    "buffer_load_dword v10, off, s[48:51], s68 sc1\n"
    "ds_read_b128 v[216:219], v38 offset:28672\n"
    "ds_read_b128 v[220:223], v38 offset:29696\n"
    "ds_read_b128 v[224:227], v38 offset:30720\n"
    "s_mul_i32 s97, s96, 29\n"
    "s_add_u32 s97, s97, s95\n"
    "Lg76_%=:\n"
    "s_memrealtime s[98:99]\n"
    "s_waitcnt lgkmcnt(0)\n"
    "s_lshl_b32 s98, s98, 4\n"
    "s_sub_u32 s98, s98, s97\n"
    "s_cmp_lt_i32 s98, 0\n"
    "s_cbranch_scc1 Lg76_%=\n"
    "s_waitcnt vmcnt(7)\n"
    "buffer_load_dwordx4 v[76:79], v0, s[40:43], s58 offen offset:1024 nt\n"
    "ds_read_b128 v[228:231], v38 offset:31744\n"
    "ds_read_b128 v[232:235], v38 offset:32768\n"
    "ds_read_b128 v[236:239], v38 offset:33792\n"
    "s_mul_i32 s97, s96, 30\n"
    "s_add_u32 s97, s97, s95\n"
    "Lg77_%=:\n"
    "s_memrealtime s[98:99]\n"
    "s_waitcnt lgkmcnt(0)\n"
    "s_lshl_b32 s98, s98, 4\n"
    "s_sub_u32 s98, s98, s97\n"
    "s_cmp_lt_i32 s98, 0\n"
    "s_cbranch_scc1 Lg77_%=\n"
    "s_waitcnt vmcnt(7)\n"
    "buffer_load_dwordx4 v[80:83], v0, s[40:43], s58 offen offset:2048 nt\n"
    "ds_read_b128 v[240:243], v38 offset:34816\n"
    "ds_read_b128 v[244:247], v38 offset:35840\n"
    "ds_read_b128 v[248:251], v38 offset:36864\n"
    "s_mul_i32 s97, s96, 31\n"
    "s_add_u32 s97, s97, s95\n"
    "Lg78_%=:\n"
    "s_memrealtime s[98:99]\n"
    "s_waitcnt lgkmcnt(0)\n"
    "s_lshl_b32 s98, s98, 4\n"
    "s_sub_u32 s98, s98, s97\n"
    "s_cmp_lt_i32 s98, 0\n"
    "s_cbranch_scc1 Lg78_%=\n"
    "s_waitcnt vmcnt(7)\n"
    "buffer_load_dwordx4 v[84:87], v0, s[40:43], s58 offen offset:3072 nt\n"
    "s_mul_i32 s97, s96, 32\n"
    "s_add_u32 s97, s97, s95\n"
    "Lg79_%=:\n"
    "s_memrealtime s[98:99]\n"
    "s_waitcnt lgkmcnt(0)\n"
    "s_lshl_b32 s98, s98, 4\n"
    "s_sub_u32 s98, s98, s97\n"
    "s_cmp_lt_i32 s98, 0\n"
    "s_cbranch_scc1 Lg79_%=\n"
    "s_waitcnt vmcnt(7)\n"
    "buffer_load_dwordx4 v[88:91], v0, s[40:43], s59 offen offset:0 nt\n"
    "s_mul_i32 s97, s96, 33\n"
    "s_add_u32 s97, s97, s95\n"
    "Lg80_%=:\n"
    "s_memrealtime s[98:99]\n"
    "s_waitcnt lgkmcnt(0)\n"
    "s_lshl_b32 s98, s98, 4\n"
    "s_sub_u32 s98, s98, s97\n"
    "s_cmp_lt_i32 s98, 0\n"
    "s_cbranch_scc1 Lg80_%=\n"
    "s_waitcnt vmcnt(7)\n"
    "buffer_load_dwordx4 v[92:95], v0, s[40:43], s59 offen offset:1024 nt\n"
    "s_mul_i32 s97, s96, 34\n"
    "s_add_u32 s97, s97, s95\n"
    "Lg81_%=:\n"
    "s_memrealtime s[98:99]\n"
    "s_waitcnt lgkmcnt(0)\n"
    "s_lshl_b32 s98, s98, 4\n"
    "s_sub_u32 s98, s98, s97\n"
    "s_cmp_lt_i32 s98, 0\n"
    "s_cbranch_scc1 Lg81_%=\n"
    "s_waitcnt vmcnt(7)\n"
    "buffer_load_dwordx4 v[96:99], v0, s[40:43], s59 offen offset:2048 nt\n"
    "s_mul_i32 s97, s96, 35\n"
    "s_add_u32 s97, s97, s95\n"
    "Lg82_%=:\n"
    "s_memrealtime s[98:99]\n"
    "s_waitcnt lgkmcnt(0)\n"
    "s_lshl_b32 s98, s98, 4\n"
    "s_sub_u32 s98, s98, s97\n"
    "s_cmp_lt_i32 s98, 0\n"
    "s_cbranch_scc1 Lg82_%=\n"
    "s_waitcnt vmcnt(7)\n"
    "buffer_load_dwordx4 v[100:103], v0, s[40:43], s59 offen offset:3072 nt\n"
    "s_mul_i32 s97, s96, 36\n"
    "s_add_u32 s97, s97, s95\n"
    "Lg83_%=:\n"
    "s_memrealtime s[98:99]\n"
    "s_waitcnt lgkmcnt(0)\n"
    "s_lshl_b32 s98, s98, 4\n"
    "s_sub_u32 s98, s98, s97\n"
    "s_cmp_lt_i32 s98, 0\n"
    "s_cbranch_scc1 Lg83_%=\n"
    "s_waitcnt vmcnt(7)\n"
    "buffer_load_dwordx4 v[104:107], v0, s[40:43], s60 offen offset:0 nt\n"
    "s_add_u32 s40, s40, 0x2000000\n"
    "s_addc_u32 s41, s41, 0\n"
    "v_readfirstlane_b32 s68, v10\n"
    "s_add_u32 s68, s68, 475\n"
    "s_lshl_b32 s68, s68, 4\n"
    "s_add_u32 s68, s68, s83\n"
    "s_sub_u32 s69, s68, s95\n"
    "s_cmp_lt_u32 s69, 24000\n"
    "s_cselect_b32 s68, s68, s95\n"
    "s_cmp_ge_u32 s61, 2\n"
    "s_cselect_b32 s95, s68, s95\n"
    "s_mul_i32 s97, s96, 37\n"
    "s_add_u32 s97, s97, s95\n"
    "Lg84_%=:\n"
    "s_memrealtime s[98:99]\n"
    "s_waitcnt lgkmcnt(0)\n"
    "s_lshl_b32 s98, s98, 4\n"
    "s_sub_u32 s98, s98, s97\n"
    "s_cmp_lt_i32 s98, 0\n"
    "s_cbranch_scc1 Lg84_%=\n"
    "s_waitcnt vmcnt(7)\n"
    "buffer_load_dwordx4 v[252:255], v1, s[44:47], s60 offen offset:0 nt\n"
    "s_mul_i32 s97, s96, 38\n"
    "s_add_u32 s97, s97, s95\n"
    "Lg85_%=:\n"
    "s_memrealtime s[98:99]\n"
    "s_waitcnt lgkmcnt(0)\n"
    "s_lshl_b32 s98, s98, 4\n"
    "s_sub_u32 s98, s98, s97\n"
    "s_cmp_lt_i32 s98, 0\n"
    "s_cbranch_scc1 Lg85_%=\n"
    "s_waitcnt vmcnt(7)\n"
    "buffer_load_dwordx4 v[108:111], v2, s[44:47], 0 offen offset:0 nt\n"
    "s_mul_i32 s97, s96, 39\n"
    "s_add_u32 s97, s97, s95\n"
    "Lg86_%=:\n"
    "s_memrealtime s[98:99]\n"
    "s_waitcnt lgkmcnt(0)\n"
    "s_lshl_b32 s98, s98, 4\n"
    "s_sub_u32 s98, s98, s97\n"
    "s_cmp_lt_i32 s98, 0\n"
    "s_cbranch_scc1 Lg86_%=\n"
    "s_waitcnt vmcnt(7)\n"
    "buffer_load_dwordx4 v[112:115], v2, s[44:47], 0 offen offset:512 nt\n"
    "s_mul_i32 s97, s96, 40\n"
    "s_add_u32 s97, s97, s95\n"
    "Lg87_%=:\n"
    "s_memrealtime s[98:99]\n"
    "s_waitcnt lgkmcnt(0)\n"
    "s_lshl_b32 s98, s98, 4\n"
    "s_sub_u32 s98, s98, s97\n"
    "s_cmp_lt_i32 s98, 0\n"
    "s_cbranch_scc1 Lg87_%=\n"
    "s_waitcnt vmcnt(7)\n"
    "buffer_load_dwordx4 v[116:119], v2, s[44:47], 0 offen offset:2048 nt\n"
    "s_mul_i32 s97, s96, 41\n"
    "s_add_u32 s97, s97, s95\n"
    "Lg88_%=:\n"
    "s_memrealtime s[98:99]\n"
    "s_waitcnt lgkmcnt(0)\n"
    "s_lshl_b32 s98, s98, 4\n"
    "s_sub_u32 s98, s98, s97\n"
    "s_cmp_lt_i32 s98, 0\n"
    "s_cbranch_scc1 Lg88_%=\n"
    "s_waitcnt vmcnt(7)\n"
    "buffer_load_dwordx4 v[120:123], v2, s[44:47], 0 offen offset:2560 nt\n"
    "s_mul_i32 s97, s96, 42\n"
    "s_add_u32 s97, s97, s95\n"
    "Lg89_%=:\n"
    "s_memrealtime s[98:99]\n"
    "s_waitcnt lgkmcnt(0)\n"
    "s_lshl_b32 s98, s98, 4\n"
    "s_sub_u32 s98, s98, s97\n"
    "s_cmp_lt_i32 s98, 0\n"
    "s_cbranch_scc1 Lg89_%=\n"
    "s_waitcnt vmcnt(7)\n"
    "buffer_load_dwordx4 v[124:127], v2, s[44:47], s52 offen offset:0 nt\n"
    "s_mul_i32 s97, s96, 43\n"
    "s_add_u32 s97, s97, s95\n"
    "Lg90_%=:\n"
    "s_memrealtime s[98:99]\n"
    "s_waitcnt lgkmcnt(0)\n"
    "s_lshl_b32 s98, s98, 4\n"
    "s_sub_u32 s98, s98, s97\n"
    "s_cmp_lt_i32 s98, 0\n"
    "s_cbranch_scc1 Lg90_%=\n"
    "s_waitcnt vmcnt(7)\n"
    "buffer_load_dwordx4 v[128:131], v2, s[44:47], s52 offen offset:512 nt\n"
    "s_mul_i32 s97, s96, 44\n"
    "s_add_u32 s97, s97, s95\n"
    "Lg91_%=:\n"
    "s_memrealtime s[98:99]\n"
    "s_waitcnt lgkmcnt(0)\n"
    "s_lshl_b32 s98, s98, 4\n"
    "s_sub_u32 s98, s98, s97\n"
    "s_cmp_lt_i32 s98, 0\n"
    "s_cbranch_scc1 Lg91_%=\n"
    "s_waitcnt vmcnt(7)\n"
    "buffer_load_dwordx4 v[132:135], v2, s[44:47], s52 offen offset:2048 nt\n"
    "s_add_u32 s68, s61, 1\n"
    "s_lshl_b32 s68, s68, 13\n"
    "s_add_u32 s68, s68, s65\n"
    "v_lshrrev_b32 v3, 6, v1\n"
    "v_lshlrev_b32 v3, 4, v3\n"
    "buffer_load_dwordx4 v[4:7], v3, s[48:51], s68 offen sc1\n"
    "s_mul_i32 s97, s96, 45\n"
    "s_add_u32 s97, s97, s95\n"
    "Lg92_%=:\n"
    "s_memrealtime s[98:99]\n"
    "s_waitcnt lgkmcnt(0)\n"
    "s_lshl_b32 s98, s98, 4\n"
    "s_sub_u32 s98, s98, s97\n"
    "s_cmp_lt_i32 s98, 0\n"
    "s_cbranch_scc1 Lg92_%=\n"
    "s_waitcnt vmcnt(7)\n"
    "buffer_load_dwordx4 v[136:139], v2, s[44:47], s52 offen offset:2560 nt\n"
    "s_mul_i32 s97, s96, 46\n"
    "s_add_u32 s97, s97, s95\n"
    "Lg93_%=:\n"
    "s_memrealtime s[98:99]\n"
    "s_waitcnt lgkmcnt(0)\n"
    "s_lshl_b32 s98, s98, 4\n"
    "s_sub_u32 s98, s98, s97\n"
    "s_cmp_lt_i32 s98, 0\n"
    "s_cbranch_scc1 Lg93_%=\n"
    "s_waitcnt vmcnt(7)\n"
    "buffer_load_dwordx4 v[140:143], v2, s[44:47], s53 offen offset:0 nt\n"
    "s_mul_i32 s97, s96, 47\n"
    "s_add_u32 s97, s97, s95\n"
    "Lg94_%=:\n"
    "s_memrealtime s[98:99]\n"
    "s_waitcnt lgkmcnt(0)\n"
    "s_lshl_b32 s98, s98, 4\n"
    "s_sub_u32 s98, s98, s97\n"
    "s_cmp_lt_i32 s98, 0\n"
    "s_cbranch_scc1 Lg94_%=\n"
    "s_waitcnt vmcnt(7)\n"
    "buffer_load_dwordx4 v[144:147], v2, s[44:47], s53 offen offset:512 nt\n"
    "s_mul_i32 s97, s96, 48\n"
    "s_add_u32 s97, s97, s95\n"
    "Lg95_%=:\n"
    "s_memrealtime s[98:99]\n"
    "s_waitcnt lgkmcnt(0)\n"
    "s_lshl_b32 s98, s98, 4\n"
    "s_sub_u32 s98, s98, s97\n"
    "s_cmp_lt_i32 s98, 0\n"
    "s_cbranch_scc1 Lg95_%=\n"
    "s_waitcnt vmcnt(7)\n"
    "buffer_load_dwordx4 v[148:151], v2, s[44:47], s53 offen offset:2048 nt\n"
    "s_mul_i32 s97, s96, 49\n"
    "s_add_u32 s97, s97, s95\n"
    "Lg96_%=:\n"
    "s_memrealtime s[98:99]\n"
    "s_waitcnt lgkmcnt(0)\n"
    "s_lshl_b32 s98, s98, 4\n"
    "s_sub_u32 s98, s98, s97\n"
    "s_cmp_lt_i32 s98, 0\n"
    "s_cbranch_scc1 Lg96_%=\n"
    "s_waitcnt vmcnt(7)\n"
    "buffer_load_dwordx4 v[152:155], v2, s[44:47], s53 offen offset:2560 nt\n"
    "s_mul_i32 s97, s96, 50\n"
    "s_add_u32 s97, s97, s95\n"
    "Lg97_%=:\n"
    "s_memrealtime s[98:99]\n"
    "s_waitcnt lgkmcnt(0)\n"
    "s_lshl_b32 s98, s98, 4\n"
    "s_sub_u32 s98, s98, s97\n"
    "s_cmp_lt_i32 s98, 0\n"
    "s_cbranch_scc1 Lg97_%=\n"
    "s_waitcnt vmcnt(7)\n"
    "buffer_load_dwordx4 v[156:159], v2, s[44:47], s54 offen offset:0 nt\n"
    "s_mul_i32 s97, s96, 51\n"
    "s_add_u32 s97, s97, s95\n"
    "Lg98_%=:\n"
    "s_memrealtime s[98:99]\n"
    "s_waitcnt lgkmcnt(0)\n"
    "s_lshl_b32 s98, s98, 4\n"
    "s_sub_u32 s98, s98, s97\n"
    "s_cmp_lt_i32 s98, 0\n"
    "s_cbranch_scc1 Lg98_%=\n"
    "s_waitcnt vmcnt(7)\n"
    "buffer_load_dwordx4 v[160:163], v2, s[44:47], s54 offen offset:512 nt\n"
    "s_waitcnt vmcnt(16)\n"
    "s_waitcnt vmcnt(7)\n"
    "s_mul_i32 s97, s96, 52\n"
    "s_add_u32 s97, s97, s95\n"
    "Lg99_%=:\n"
    "s_memrealtime s[98:99]\n"
    "s_waitcnt lgkmcnt(0)\n"
    "s_lshl_b32 s98, s98, 4\n"
    "s_sub_u32 s98, s98, s97\n"
    "s_cmp_lt_i32 s98, 0\n"
    "s_cbranch_scc1 Lg99_%=\n"
    "s_waitcnt vmcnt(7)\n"
    "buffer_load_dwordx4 v[164:167], v2, s[44:47], s54 offen offset:2048 nt\n"
    "s_waitcnt lgkmcnt(0)\n"
    "Lqdc_%=:\n"
    "v_max3_u32 v3, v4, v5, v6\n"
    "v_max_u32 v3, v3, v7\n"
    "v_cmp_eq_u32 vcc, -1, v3\n"
    "s_cbranch_vccz Lqdd_%=\n"
    "s_nop 3\n"
    "Lqdr_%=:\n"
    "s_sub_u32 s67, s67, 1\n"
    "s_cmp_lt_i32 s67, 0\n"
    "s_cbranch_scc1 Lqdd_%=\n"
    "s_add_u32 s68, s61, 1\n"
    "s_lshl_b32 s68, s68, 13\n"
    "s_add_u32 s68, s68, s65\n"
    "v_lshrrev_b32 v3, 6, v1\n"
    "v_lshlrev_b32 v3, 4, v3\n"
    "buffer_load_dwordx4 v[4:7], v3, s[48:51], s68 offen sc1\n"
    "s_memrealtime s[98:99]\n"
    "s_mul_i32 s97, s96, 53\n"
    "s_waitcnt lgkmcnt(0)\n"
    "s_lshl_b32 s98, s98, 4\n"
    "s_sub_u32 s98, s98, s97\n"
    "s_sub_u32 s97, s98, s95\n"
    "s_cmp_gt_i32 s97, 0\n"
    "s_cselect_b32 s95, s98, s95\n"
    "s_waitcnt vmcnt(0)\n"
    "s_branch Lqdc_%=\n"
    "Lqdd_%=:\n"
    "v_cndmask_b32 v8, 0, v4, s[70:71]\n"
    "v_cndmask_b32 v9, 0, v5, s[70:71]\n"
    "v_cndmask_b32 v10, 0, v6, s[70:71]\n"
    "v_cndmask_b32 v11, 0, v7, s[70:71]\n"
    "v_cndmask_b32 v12, 0, v4, s[72:73]\n"
    "v_cndmask_b32 v13, 0, v5, s[72:73]\n"
    "v_cndmask_b32 v14, 0, v6, s[72:73]\n"
    "v_cndmask_b32 v15, 0, v7, s[72:73]\n"
    "v_cndmask_b32 v16, 0, v4, s[74:75]\n"
    "v_cndmask_b32 v17, 0, v5, s[74:75]\n"
    "v_cndmask_b32 v18, 0, v6, s[74:75]\n"
    "v_cndmask_b32 v19, 0, v7, s[74:75]\n"
    "v_cndmask_b32 v4, 0, v4, s[76:77]\n"
    "v_cndmask_b32 v5, 0, v5, s[76:77]\n"
    "v_cndmask_b32 v6, 0, v6, s[76:77]\n"
    "v_cndmask_b32 v7, 0, v7, s[76:77]\n"
    "v_mfma_f32_16x16x32_bf16 v[20:23], v[192:195], v[8:11], 0\n"
    "v_mfma_f32_16x16x32_bf16 v[20:23], v[196:199], v[12:15], v[20:23]\n"
    "v_mfma_f32_16x16x32_bf16 v[20:23], v[200:203], v[16:19], v[20:23]\n"
    "v_mfma_f32_16x16x32_bf16 v[20:23], v[204:207], v[4:7], v[20:23]\n"
    "v_mfma_f32_16x16x32_bf16 v[24:27], v[208:211], v[8:11], 0\n"
    "v_mfma_f32_16x16x32_bf16 v[24:27], v[212:215], v[12:15], v[24:27]\n"
    "v_mfma_f32_16x16x32_bf16 v[24:27], v[216:219], v[16:19], v[24:27]\n"
    "v_mfma_f32_16x16x32_bf16 v[24:27], v[220:223], v[4:7], v[24:27]\n"
    "v_mfma_f32_16x16x32_bf16 v[28:31], v[224:227], v[8:11], 0\n"
    "buffer_load_dwordx4 v[168:171], v2, s[44:47], s54 offen offset:2560 nt\n"
    "s_nop 1\n"
    "v_add_f32_dpp v36, v21, v20 row_ror:12 row_mask:0xf bank_mask:0x1\n"
    "v_mfma_f32_16x16x32_bf16 v[28:31], v[228:231], v[12:15], v[28:31]\n"
    "s_nop 0\n"
    "v_add_f32_dpp v36, v22, v36 row_ror:8 row_mask:0xf bank_mask:0x1\n"
    "v_mfma_f32_16x16x32_bf16 v[28:31], v[232:235], v[16:19], v[28:31]\n"
    "s_nop 0\n"
    "v_add_f32_dpp v36, v23, v36 row_ror:4 row_mask:0xf bank_mask:0x1\n"
    "v_mfma_f32_16x16x32_bf16 v[28:31], v[236:239], v[4:7], v[28:31]\n"
    "v_mfma_f32_16x16x32_bf16 v[20:23], v[240:243], v[8:11], 0\n"
    "v_add_f32_dpp v36, v26, v25 row_ror:12 row_mask:0xf bank_mask:0x2\n"
    "v_mfma_f32_16x16x32_bf16 v[20:23], v[244:247], v[12:15], v[20:23]\n"
    "s_nop 0\n"
    "v_add_f32_dpp v36, v27, v36 row_ror:8 row_mask:0xf bank_mask:0x2\n"
    "v_mfma_f32_16x16x32_bf16 v[20:23], v[248:251], v[16:19], v[20:23]\n"
    "s_nop 0\n"
    "v_add_f32_dpp v36, v24, v36 row_ror:4 row_mask:0xf bank_mask:0x2\n"
    "v_mfma_f32_16x16x32_bf16 v[20:23], a[236:239], v[4:7], v[20:23]\n"
    "v_mfma_f32_16x16x32_bf16 v[24:27], a[240:243], v[8:11], 0\n"
    "v_add_f32_dpp v36, v31, v30 row_ror:12 row_mask:0xf bank_mask:0x4\n"
    "v_mfma_f32_16x16x32_bf16 v[24:27], a[244:247], v[12:15], v[24:27]\n"
    "s_nop 0\n"
    "v_add_f32_dpp v36, v28, v36 row_ror:8 row_mask:0xf bank_mask:0x4\n"
    "v_mfma_f32_16x16x32_bf16 v[24:27], a[248:251], v[16:19], v[24:27]\n"
    "buffer_load_dwordx4 v[172:175], v2, s[44:47], s55 offen offset:0 nt\n"
    "v_add_f32_dpp v36, v29, v36 row_ror:4 row_mask:0xf bank_mask:0x4\n"
    "v_mfma_f32_16x16x32_bf16 v[24:27], a[252:255], v[4:7], v[24:27]\n"
    "v_mfma_f32_16x16x32_bf16 v[28:31], v[40:43], v[8:11], 0\n"
    "v_add_f32_dpp v36, v20, v23 row_ror:12 row_mask:0xf bank_mask:0x8\n"
    "v_mfma_f32_16x16x32_bf16 v[28:31], v[44:47], v[12:15], v[28:31]\n"
    "s_nop 0\n"
    "v_add_f32_dpp v36, v21, v36 row_ror:8 row_mask:0xf bank_mask:0x8\n"
    "v_mfma_f32_16x16x32_bf16 v[28:31], v[48:51], v[16:19], v[28:31]\n"
    "s_nop 0\n"
    "v_add_f32_dpp v36, v22, v36 row_ror:4 row_mask:0xf bank_mask:0x8\n"
    "v_mfma_f32_16x16x32_bf16 v[28:31], v[52:55], v[4:7], v[28:31]\n"
    "v_mfma_f32_16x16x32_bf16 v[20:23], v[56:59], v[8:11], 0\n"
    "v_add_f32_dpp v37, v25, v24 row_ror:12 row_mask:0xf bank_mask:0x1\n"
    "v_mfma_f32_16x16x32_bf16 v[20:23], v[60:63], v[12:15], v[20:23]\n"
    "s_nop 0\n"
    "v_add_f32_dpp v37, v26, v37 row_ror:8 row_mask:0xf bank_mask:0x1\n"
    "v_mfma_f32_16x16x32_bf16 v[20:23], v[64:67], v[16:19], v[20:23]\n"
    "s_nop 0\n"
    "v_add_f32_dpp v37, v27, v37 row_ror:4 row_mask:0xf bank_mask:0x1\n"
    "v_mfma_f32_16x16x32_bf16 v[20:23], v[68:71], v[4:7], v[20:23]\n"
    "v_mfma_f32_16x16x32_bf16 v[24:27], v[72:75], v[8:11], 0\n"
    "buffer_load_dwordx4 v[176:179], v2, s[44:47], s55 offen offset:512 nt\n"
    "v_add_f32_dpp v37, v30, v29 row_ror:12 row_mask:0xf bank_mask:0x2\n"
    "v_mfma_f32_16x16x32_bf16 v[24:27], v[76:79], v[12:15], v[24:27]\n"
    "s_nop 0\n"
    "v_add_f32_dpp v37, v31, v37 row_ror:8 row_mask:0xf bank_mask:0x2\n"
    "v_mfma_f32_16x16x32_bf16 v[24:27], v[80:83], v[16:19], v[24:27]\n"
    "s_nop 0\n"
    "v_add_f32_dpp v37, v28, v37 row_ror:4 row_mask:0xf bank_mask:0x2\n"
    "v_mfma_f32_16x16x32_bf16 v[24:27], v[84:87], v[4:7], v[24:27]\n"
    "v_mfma_f32_16x16x32_bf16 v[28:31], v[88:91], v[8:11], 0\n"
    "v_add_f32_dpp v37, v23, v22 row_ror:12 row_mask:0xf bank_mask:0x4\n"
    "v_mfma_f32_16x16x32_bf16 v[28:31], v[92:95], v[12:15], v[28:31]\n"
    "s_nop 0\n"
    "v_add_f32_dpp v37, v20, v37 row_ror:8 row_mask:0xf bank_mask:0x4\n"
    "v_mfma_f32_16x16x32_bf16 v[28:31], v[96:99], v[16:19], v[28:31]\n"
    "s_nop 0\n"
    "v_add_f32_dpp v37, v21, v37 row_ror:4 row_mask:0xf bank_mask:0x4\n"
    "v_mfma_f32_16x16x32_bf16 v[28:31], v[100:103], v[4:7], v[28:31]\n"
    "v_mfma_f32_16x16x32_bf16 v[20:23], v[104:107], v[8:11], 0\n"
    "v_add_f32_dpp v37, v24, v27 row_ror:12 row_mask:0xf bank_mask:0x8\n"
    "s_nop 1\n"
    "v_add_f32_dpp v37, v25, v37 row_ror:8 row_mask:0xf bank_mask:0x8\n"
    "s_nop 1\n"
    "v_add_f32_dpp v37, v26, v37 row_ror:4 row_mask:0xf bank_mask:0x8\n"
    "v_add_f32_dpp v38, v29, v28 row_ror:12 row_mask:0xf bank_mask:0x1\n"
    "s_nop 1\n"
    "v_add_f32_dpp v38, v30, v38 row_ror:8 row_mask:0xf bank_mask:0x1\n"
    "s_nop 1\n"
    "v_add_f32_dpp v38, v31, v38 row_ror:4 row_mask:0xf bank_mask:0x1\n"
    "s_nop 1\n"
    "v_add_f32_dpp v38, v22, v21 row_ror:12 row_mask:0xf bank_mask:0x2\n"
    "s_nop 1\n"
    "v_add_f32_dpp v38, v23, v38 row_ror:8 row_mask:0xf bank_mask:0x2\n"
    "s_nop 1\n"
    "v_add_f32_dpp v38, v20, v38 row_ror:4 row_mask:0xf bank_mask:0x2\n"
    "v_cvt_pk_bf16_f32 v8, v36, v37\n"
    "v_cvt_pk_bf16_f32 v9, v38, v38\n"
    "v_lshlrev_b32 v36, 16, v8\n"
    "v_and_b32 v37, s87, v8\n"
    "v_and_b32 v38, s87, v9\n"
    "v_mul_f32 v36, s84, v36\n"
    "v_mul_f32 v37, s84, v37\n"
    "v_mul_f32 v38, s84, v38\n"
    "v_cvt_pk_bf16_f32 v8, v36, v37\n"
    "v_cvt_pk_bf16_f32 v9, v38, v38\n"
    "v_mov_b32 v10, s86\n"
    "v_lshlrev_b32 v36, 16, v8\n"
    "v_and_b32 v37, s87, v8\n"
    "v_and_b32 v38, s87, v9\n"
    "v_cndmask_b32 v38, v10, v38, s[78:79]\n"
    "v_max3_f32 v8, v36, v37, v38\n"
    "buffer_load_dwordx4 v[180:183], v2, s[44:47], s55 offen offset:2048 nt\n"
    "buffer_load_dwordx4 v[184:187], v2, s[44:47], s55 offen offset:2560 nt\n"
    "v_max_f32_dpp v8, v8, v8 row_ror:8 row_mask:0xf bank_mask:0xf\n"
    "s_nop 1\n"
    "v_max_f32_dpp v8, v8, v8 row_ror:4 row_mask:0xf bank_mask:0xf\n"
    "s_nop 1\n"
    "v_max_f32_dpp v8, v8, v8 row_ror:2 row_mask:0xf bank_mask:0xf\n"
    "s_nop 1\n"
    "v_max_f32_dpp v9, v8, v8 row_ror:1 row_mask:0xf bank_mask:0xf\n"
    "v_max_f32_dpp v8, v8, v8 row_ror:1 row_mask:0xf bank_mask:0xf\n"
    "s_nop 1\n"
    "v_permlane16_swap_b32 v8, v9\n"
    "v_max_f32 v10, v8, v9\n"
    "v_max_f32 v8, v8, v9\n"
    "s_nop 1\n"
    "v_permlane32_swap_b32 v8, v10\n"
    "v_max_f32 v8, v8, v10\n"
    "v_mul_f32 v16, s85, v8\n"
    "v_fma_f32 v38, v38, s85, -v16\n"
    "v_fma_f32 v36, v36, s85, -v16\n"
    "v_fma_f32 v37, v37, s85, -v16\n"
    "v_exp_f32 v38, v38\n"
    "v_exp_f32 v36, v36\n"
    "v_exp_f32 v37, v37\n"
    "v_add_f32 v17, v38, v36\n"
    "v_cvt_pk_bf16_f32 v38, v38, v38\n"
    "v_add_f32 v17, v17, v37\n"
    "v_cvt_pk_bf16_f32 v36, v36, v36\n"
    "v_cvt_pk_bf16_f32 v37, v37, v37\n"
    "v_and_b32_dpp v19, v38, v35 row_newbcast:4 row_mask:0xf bank_mask:0xf\n"
    "s_mov_b32 vcc_lo, 0x3030303\n"
    "s_mov_b32 vcc_hi, 0x3030303\n"
    "v_cndmask_b32 v8, 0, v19, vcc\n"
    "s_mov_b32 vcc_lo, 0xc0c0c0c\n"
    "s_mov_b32 vcc_hi, 0xc0c0c0c\n"
    "v_cndmask_b32 v9, 0, v19, vcc\n"
    "s_mov_b32 vcc_lo, 0x30303030\n"
    "s_mov_b32 vcc_hi, 0x30303030\n"
    "v_cndmask_b32 v10, 0, v19, vcc\n"
    "s_mov_b32 vcc_lo, 0xc0c0c0c0\n"
    "s_mov_b32 vcc_hi, 0xc0c0c0c0\n"
    "v_cndmask_b32 v11, 0, v19, vcc\n"
    "s_mul_i32 s97, s96, 58\n"
    "s_add_u32 s97, s97, s95\n"
    "Lg100_%=:\n"
    "s_memrealtime s[98:99]\n"
    "s_waitcnt lgkmcnt(0)\n"
    "s_lshl_b32 s98, s98, 4\n"
    "s_sub_u32 s98, s98, s97\n"
    "s_cmp_lt_i32 s98, 0\n"
    "s_cbranch_scc1 Lg100_%=\n"
    "s_waitcnt vmcnt(7)\n"
    "buffer_load_dwordx4 v[188:191], v2, s[44:47], s56 offen offset:0 nt\n"
    "v_mfma_f32_16x16x32_bf16 v[28:31], v[8:11], v[252:255], 0\n"
    "v_and_b32_dpp v12, v36, v32 row_newbcast:0 row_mask:0xf bank_mask:0xf\n"
    "v_and_b32_dpp v13, v36, v33 row_newbcast:0 row_mask:0xf bank_mask:0xf\n"
    "v_and_b32_dpp v14, v36, v32 row_newbcast:1 row_mask:0xf bank_mask:0xf\n"
    "v_and_b32_dpp v15, v36, v33 row_newbcast:1 row_mask:0xf bank_mask:0xf\n"
    "v_add_f32_dpp v17, v17, v17 row_ror:8 row_mask:0xf bank_mask:0xf\n"
    "s_nop 0\n"
    "v_smfmac_f32_16x16x64_bf16 v[28:31], v[12:15], v[108:115], v34\n"
    "v_and_b32_dpp v8, v36, v32 row_newbcast:2 row_mask:0xf bank_mask:0xf\n"
    "v_and_b32_dpp v9, v36, v33 row_newbcast:2 row_mask:0xf bank_mask:0xf\n"
    "v_and_b32_dpp v10, v36, v32 row_newbcast:3 row_mask:0xf bank_mask:0xf\n"
    "v_and_b32_dpp v11, v36, v33 row_newbcast:3 row_mask:0xf bank_mask:0xf\n"
    "v_add_f32_dpp v17, v17, v17 row_ror:4 row_mask:0xf bank_mask:0xf\n"
    "s_mul_i32 s97, s96, 59\n"
    "s_add_u32 s97, s97, s95\n"
    "Lg101_%=:\n"
    "s_memrealtime s[98:99]\n"
    "s_waitcnt lgkmcnt(0)\n"
    "s_lshl_b32 s98, s98, 4\n"
    "s_sub_u32 s98, s98, s97\n"
    "s_cmp_lt_i32 s98, 0\n"
    "s_cbranch_scc1 Lg101_%=\n"
    "s_waitcnt vmcnt(7)\n"
    "buffer_load_dwordx4 v[192:195], v2, s[44:47], s56 offen offset:512 nt\n"
    "v_smfmac_f32_16x16x64_bf16 v[28:31], v[8:11], v[116:123], v34\n"
    "v_and_b32_dpp v12, v36, v32 row_newbcast:4 row_mask:0xf bank_mask:0xf\n"
    "v_and_b32_dpp v13, v36, v33 row_newbcast:4 row_mask:0xf bank_mask:0xf\n"
    "v_and_b32_dpp v14, v36, v32 row_newbcast:5 row_mask:0xf bank_mask:0xf\n"
    "v_and_b32_dpp v15, v36, v33 row_newbcast:5 row_mask:0xf bank_mask:0xf\n"
    "v_add_f32_dpp v17, v17, v17 row_ror:2 row_mask:0xf bank_mask:0xf\n"
    "s_nop 0\n"
    "v_smfmac_f32_16x16x64_bf16 v[28:31], v[12:15], v[124:131], v34\n"
    "v_and_b32_dpp v8, v36, v32 row_newbcast:6 row_mask:0xf bank_mask:0xf\n"
    "v_and_b32_dpp v9, v36, v33 row_newbcast:6 row_mask:0xf bank_mask:0xf\n"
    "v_and_b32_dpp v10, v36, v32 row_newbcast:7 row_mask:0xf bank_mask:0xf\n"
    "v_and_b32_dpp v11, v36, v33 row_newbcast:7 row_mask:0xf bank_mask:0xf\n"
    "v_add_f32_dpp v17, v17, v17 row_ror:1 row_mask:0xf bank_mask:0xf\n"
    "s_mul_i32 s97, s96, 60\n"
    "s_add_u32 s97, s97, s95\n"
    "Lg102_%=:\n"
    "s_memrealtime s[98:99]\n"
    "s_waitcnt lgkmcnt(0)\n"
    "s_lshl_b32 s98, s98, 4\n"
    "s_sub_u32 s98, s98, s97\n"
    "s_cmp_lt_i32 s98, 0\n"
    "s_cbranch_scc1 Lg102_%=\n"
    "s_waitcnt vmcnt(7)\n"
    "buffer_load_dwordx4 v[196:199], v2, s[44:47], s56 offen offset:2048 nt\n"
    "v_smfmac_f32_16x16x64_bf16 v[28:31], v[8:11], v[132:139], v34\n"
    "v_and_b32_dpp v12, v36, v32 row_newbcast:8 row_mask:0xf bank_mask:0xf\n"
    "v_and_b32_dpp v13, v36, v33 row_newbcast:8 row_mask:0xf bank_mask:0xf\n"
    "v_and_b32_dpp v14, v36, v32 row_newbcast:9 row_mask:0xf bank_mask:0xf\n"
    "v_and_b32_dpp v15, v36, v33 row_newbcast:9 row_mask:0xf bank_mask:0xf\n"
    "v_mov_b32 v18, v17\n"
    "s_nop 0\n"
    "v_smfmac_f32_16x16x64_bf16 v[28:31], v[12:15], v[140:147], v34\n"
    "v_and_b32_dpp v8, v36, v32 row_newbcast:10 row_mask:0xf bank_mask:0xf\n"
    "v_and_b32_dpp v9, v36, v33 row_newbcast:10 row_mask:0xf bank_mask:0xf\n"
    "v_and_b32_dpp v10, v36, v32 row_newbcast:11 row_mask:0xf bank_mask:0xf\n"
    "v_and_b32_dpp v11, v36, v33 row_newbcast:11 row_mask:0xf bank_mask:0xf\n"
    "v_permlane16_swap_b32 v17, v18\n"
    "v_add_f32 v17, v17, v18\n"
    "s_mul_i32 s97, s96, 61\n"
    "s_add_u32 s97, s97, s95\n"
    "Lg103_%=:\n"
    "s_memrealtime s[98:99]\n"
    "s_waitcnt lgkmcnt(0)\n"
    "s_lshl_b32 s98, s98, 4\n"
    "s_sub_u32 s98, s98, s97\n"
    "s_cmp_lt_i32 s98, 0\n"
    "s_cbranch_scc1 Lg103_%=\n"
    "s_waitcnt vmcnt(7)\n"
    "buffer_load_dwordx4 v[200:203], v2, s[44:47], s56 offen offset:2560 nt\n"
    "v_smfmac_f32_16x16x64_bf16 v[28:31], v[8:11], v[148:155], v34\n"
    "v_and_b32_dpp v12, v36, v32 row_newbcast:12 row_mask:0xf bank_mask:0xf\n"
    "v_and_b32_dpp v13, v36, v33 row_newbcast:12 row_mask:0xf bank_mask:0xf\n"
    "v_and_b32_dpp v14, v36, v32 row_newbcast:13 row_mask:0xf bank_mask:0xf\n"
    "v_and_b32_dpp v15, v36, v33 row_newbcast:13 row_mask:0xf bank_mask:0xf\n"
    "v_mov_b32 v18, v17\n"
    "s_nop 0\n"
    "v_smfmac_f32_16x16x64_bf16 v[28:31], v[12:15], v[156:163], v34\n"
    "v_and_b32_dpp v8, v36, v32 row_newbcast:14 row_mask:0xf bank_mask:0xf\n"
    "v_and_b32_dpp v9, v36, v33 row_newbcast:14 row_mask:0xf bank_mask:0xf\n"
    "v_and_b32_dpp v10, v36, v32 row_newbcast:15 row_mask:0xf bank_mask:0xf\n"
    "v_and_b32_dpp v11, v36, v33 row_newbcast:15 row_mask:0xf bank_mask:0xf\n"
    "v_permlane32_swap_b32 v17, v18\n"
    "v_add_f32 v17, v17, v18\n"
    "s_mul_i32 s97, s96, 62\n"
    "s_add_u32 s97, s97, s95\n"
    "Lg104_%=:\n"
    "s_memrealtime s[98:99]\n"
    "s_waitcnt lgkmcnt(0)\n"
    "s_lshl_b32 s98, s98, 4\n"
    "s_sub_u32 s98, s98, s97\n"
    "s_cmp_lt_i32 s98, 0\n"
    "s_cbranch_scc1 Lg104_%=\n"
    "s_waitcnt vmcnt(7)\n"
    "buffer_load_dwordx4 v[204:207], v2, s[44:47], s57 offen offset:0 nt\n"
    "v_smfmac_f32_16x16x64_bf16 v[28:31], v[8:11], v[164:171], v34\n"
    "v_and_b32_dpp v12, v37, v32 row_newbcast:0 row_mask:0xf bank_mask:0xf\n"
    "v_and_b32_dpp v13, v37, v33 row_newbcast:0 row_mask:0xf bank_mask:0xf\n"
    "v_and_b32_dpp v14, v37, v32 row_newbcast:1 row_mask:0xf bank_mask:0xf\n"
    "v_and_b32_dpp v15, v37, v33 row_newbcast:1 row_mask:0xf bank_mask:0xf\n"
    "v_mov_b32 v18, s82\n"
    "s_mov_b64 exec, 1\n"
    "ds_write_b32 v18, v16 offset:4160\n"
    "ds_write_b32 v18, v17 offset:4176\n"
    "s_mov_b64 exec, -1\n"
    "s_mul_i32 s97, s96, 63\n"
    "s_add_u32 s97, s97, s95\n"
    "Lg105_%=:\n"
    "s_memrealtime s[98:99]\n"
    "s_waitcnt lgkmcnt(0)\n"
    "s_lshl_b32 s98, s98, 4\n"
    "s_sub_u32 s98, s98, s97\n"
    "s_cmp_lt_i32 s98, 0\n"
    "s_cbranch_scc1 Lg105_%=\n"
    "s_waitcnt vmcnt(7)\n"
    "buffer_load_dwordx4 v[208:211], v2, s[44:47], s57 offen offset:512 nt\n"
    "v_smfmac_f32_16x16x64_bf16 v[28:31], v[12:15], v[172:179], v34\n"
    "v_and_b32_dpp v8, v37, v32 row_newbcast:2 row_mask:0xf bank_mask:0xf\n"
    "v_and_b32_dpp v9, v37, v33 row_newbcast:2 row_mask:0xf bank_mask:0xf\n"
    "v_and_b32_dpp v10, v37, v32 row_newbcast:3 row_mask:0xf bank_mask:0xf\n"
    "v_and_b32_dpp v11, v37, v33 row_newbcast:3 row_mask:0xf bank_mask:0xf\n"
    "s_mul_i32 s97, s96, 64\n"
    "s_add_u32 s97, s97, s95\n"
    "Lg106_%=:\n"
    "s_memrealtime s[98:99]\n"
    "s_waitcnt lgkmcnt(0)\n"
    "s_lshl_b32 s98, s98, 4\n"
    "s_sub_u32 s98, s98, s97\n"
    "s_cmp_lt_i32 s98, 0\n"
    "s_cbranch_scc1 Lg106_%=\n"
    "s_waitcnt vmcnt(7)\n"
    "buffer_load_dwordx4 v[212:215], v2, s[44:47], s57 offen offset:2048 nt\n"
    "s_mul_i32 s97, s96, 65\n"
    "s_add_u32 s97, s97, s95\n"
    "Lg107_%=:\n"
    "s_memrealtime s[98:99]\n"
    "s_waitcnt lgkmcnt(0)\n"
    "s_lshl_b32 s98, s98, 4\n"
    "s_sub_u32 s98, s98, s97\n"
    "s_cmp_lt_i32 s98, 0\n"
    "s_cbranch_scc1 Lg107_%=\n"
    "s_waitcnt vmcnt(7)\n"
    "buffer_load_dwordx4 v[216:219], v2, s[44:47], s57 offen offset:2560 nt\n"
    "v_smfmac_f32_16x16x64_bf16 v[28:31], v[8:11], v[180:187], v34\n"
    "v_and_b32_dpp v12, v37, v32 row_newbcast:4 row_mask:0xf bank_mask:0xf\n"
    "v_and_b32_dpp v13, v37, v33 row_newbcast:4 row_mask:0xf bank_mask:0xf\n"
    "v_and_b32_dpp v14, v37, v32 row_newbcast:5 row_mask:0xf bank_mask:0xf\n"
    "v_and_b32_dpp v15, v37, v33 row_newbcast:5 row_mask:0xf bank_mask:0xf\n"
    "s_mul_i32 s97, s96, 66\n"
    "s_add_u32 s97, s97, s95\n"
    "Lg108_%=:\n"
    "s_memrealtime s[98:99]\n"
    "s_waitcnt lgkmcnt(0)\n"
    "s_lshl_b32 s98, s98, 4\n"
    "s_sub_u32 s98, s98, s97\n"
    "s_cmp_lt_i32 s98, 0\n"
    "s_cbranch_scc1 Lg108_%=\n"
    "s_waitcnt vmcnt(7)\n"
    "buffer_load_dwordx4 v[220:223], v2, s[44:47], s58 offen offset:0 nt\n"
    "s_mul_i32 s97, s96, 67\n"
    "s_add_u32 s97, s97, s95\n"
    "Lg109_%=:\n"
    "s_memrealtime s[98:99]\n"
    "s_waitcnt lgkmcnt(0)\n"
    "s_lshl_b32 s98, s98, 4\n"
    "s_sub_u32 s98, s98, s97\n"
    "s_cmp_lt_i32 s98, 0\n"
    "s_cbranch_scc1 Lg109_%=\n"
    "s_waitcnt vmcnt(7)\n"
    "buffer_load_dwordx4 v[224:227], v2, s[44:47], s58 offen offset:512 nt\n"
    "v_smfmac_f32_16x16x64_bf16 v[28:31], v[12:15], v[188:195], v34\n"
    "v_and_b32_dpp v8, v37, v32 row_newbcast:6 row_mask:0xf bank_mask:0xf\n"
    "v_and_b32_dpp v9, v37, v33 row_newbcast:6 row_mask:0xf bank_mask:0xf\n"
    "v_and_b32_dpp v10, v37, v32 row_newbcast:7 row_mask:0xf bank_mask:0xf\n"
    "v_and_b32_dpp v11, v37, v33 row_newbcast:7 row_mask:0xf bank_mask:0xf\n"
    "s_mul_i32 s97, s96, 68\n"
    "s_add_u32 s97, s97, s95\n"
    "Lg110_%=:\n"
    "s_memrealtime s[98:99]\n"
    "s_waitcnt lgkmcnt(0)\n"
    "s_lshl_b32 s98, s98, 4\n"
    "s_sub_u32 s98, s98, s97\n"
    "s_cmp_lt_i32 s98, 0\n"
    "s_cbranch_scc1 Lg110_%=\n"
    "s_waitcnt vmcnt(7)\n"
    "buffer_load_dwordx4 v[228:231], v2, s[44:47], s58 offen offset:2048 nt\n"
    "s_mul_i32 s97, s96, 69\n"
    "s_add_u32 s97, s97, s95\n"
    "Lg111_%=:\n"
    "s_memrealtime s[98:99]\n"
    "s_waitcnt lgkmcnt(0)\n"
    "s_lshl_b32 s98, s98, 4\n"
    "s_sub_u32 s98, s98, s97\n"
    "s_cmp_lt_i32 s98, 0\n"
    "s_cbranch_scc1 Lg111_%=\n"
    "s_waitcnt vmcnt(7)\n"
    "buffer_load_dwordx4 v[232:235], v2, s[44:47], s58 offen offset:2560 nt\n"
    "v_smfmac_f32_16x16x64_bf16 v[28:31], v[8:11], v[196:203], v34\n"
    "v_and_b32_dpp v12, v37, v32 row_newbcast:8 row_mask:0xf bank_mask:0xf\n"
    "v_and_b32_dpp v13, v37, v33 row_newbcast:8 row_mask:0xf bank_mask:0xf\n"
    "v_and_b32_dpp v14, v37, v32 row_newbcast:9 row_mask:0xf bank_mask:0xf\n"
    "v_and_b32_dpp v15, v37, v33 row_newbcast:9 row_mask:0xf bank_mask:0xf\n"
    "s_mul_i32 s97, s96, 70\n"
    "s_add_u32 s97, s97, s95\n"
    "Lg112_%=:\n"
    "s_memrealtime s[98:99]\n"
    "s_waitcnt lgkmcnt(0)\n"
    "s_lshl_b32 s98, s98, 4\n"
    "s_sub_u32 s98, s98, s97\n"
    "s_cmp_lt_i32 s98, 0\n"
    "s_cbranch_scc1 Lg112_%=\n"
    "s_waitcnt vmcnt(7)\n"
    "buffer_load_dwordx4 v[236:239], v2, s[44:47], s59 offen offset:0 nt\n"
    "s_mul_i32 s97, s96, 71\n"
    "s_add_u32 s97, s97, s95\n"
    "Lg113_%=:\n"
    "s_memrealtime s[98:99]\n"
    "s_waitcnt lgkmcnt(0)\n"
    "s_lshl_b32 s98, s98, 4\n"
    "s_sub_u32 s98, s98, s97\n"
    "s_cmp_lt_i32 s98, 0\n"
    "s_cbranch_scc1 Lg113_%=\n"
    "s_waitcnt vmcnt(7)\n"
    "buffer_load_dwordx4 v[240:243], v2, s[44:47], s59 offen offset:512 nt\n"
    "v_smfmac_f32_16x16x64_bf16 v[28:31], v[12:15], v[204:211], v34\n"
    "v_and_b32_dpp v8, v37, v32 row_newbcast:10 row_mask:0xf bank_mask:0xf\n"
    "v_and_b32_dpp v9, v37, v33 row_newbcast:10 row_mask:0xf bank_mask:0xf\n"
    "v_and_b32_dpp v10, v37, v32 row_newbcast:11 row_mask:0xf bank_mask:0xf\n"
    "v_and_b32_dpp v11, v37, v33 row_newbcast:11 row_mask:0xf bank_mask:0xf\n"
    "s_mul_i32 s97, s96, 72\n"
    "s_add_u32 s97, s97, s95\n"
    "Lg114_%=:\n"
    "s_memrealtime s[98:99]\n"
    "s_waitcnt lgkmcnt(0)\n"
    "s_lshl_b32 s98, s98, 4\n"
    "s_sub_u32 s98, s98, s97\n"
    "s_cmp_lt_i32 s98, 0\n"
    "s_cbranch_scc1 Lg114_%=\n"
    "s_waitcnt vmcnt(7)\n"
    "buffer_load_dwordx4 v[244:247], v2, s[44:47], s59 offen offset:2048 nt\n"
    "s_mul_i32 s97, s96, 73\n"
    "s_add_u32 s97, s97, s95\n"
    "Lg115_%=:\n"
    "s_memrealtime s[98:99]\n"
    "s_waitcnt lgkmcnt(0)\n"
    "s_lshl_b32 s98, s98, 4\n"
    "s_sub_u32 s98, s98, s97\n"
    "s_cmp_lt_i32 s98, 0\n"
    "s_cbranch_scc1 Lg115_%=\n"
    "s_waitcnt vmcnt(7)\n"
    "buffer_load_dwordx4 v[248:251], v2, s[44:47], s59 offen offset:2560 nt\n"
    "s_add_u32 s44, s44, 0x2000000\n"
    "s_addc_u32 s45, s45, 0\n"
    "v_smfmac_f32_16x16x64_bf16 v[28:31], v[8:11], v[212:219], v34\n"
    "v_and_b32_dpp v12, v37, v32 row_newbcast:12 row_mask:0xf bank_mask:0xf\n"
    "v_and_b32_dpp v13, v37, v33 row_newbcast:12 row_mask:0xf bank_mask:0xf\n"
    "v_and_b32_dpp v14, v37, v32 row_newbcast:13 row_mask:0xf bank_mask:0xf\n"
    "v_and_b32_dpp v15, v37, v33 row_newbcast:13 row_mask:0xf bank_mask:0xf\n"
    "s_mul_i32 s97, s96, 74\n"
    "s_add_u32 s95, s95, s97\n"
    "s_mov_b32 s96, 200\n"
    "s_add_u32 s61, s61, 2\n"
    "s_mov_b32 s68, s62\n"
    "s_mov_b32 s62, s63\n"
    "s_mov_b32 s63, s68\n"
    "s_cmp_lt_u32 s61, 8\n"
    "s_cbranch_scc1 Ltop_%=\n"
    "s_mov_b32 s42, 0\n"
    "s_mov_b32 s46, 0\n"
    "s_mov_b32 s96, 0\n"
    "s_sub_u32 s95, s95, 1600\n"
    "s_branch Ltop_%=\n"
    "Lsetup_%=:\n"
    "s_load_dwordx2 s[92:93], %2, 970752\n"
    "s_getpc_b64 s[68:69]\n"
    "Lpc_%=:\n"
    "s_sub_u32 s68, s68, Lpc_%=-Ltop_%=\n"
    "s_subb_u32 s69, s69, 0\n"
    "s_lshr_b32 s97, %9, 15\n"
    "s_lshl_b32 s97, s97, 2\n"
    "s_add_u32 s97, s97, %8\n"
    "s_and_b32 s97, s97, 63\n"
    "s_lshl_b32 s97, s97, 10\n"
    "s_min_i32 s97, s97, Lfused_code_end-Ltop_%=-1024\n"
    "s_max_i32 s97, s97, 0\n"
    "v_add_u32 v9, s97, v1\n"
    "global_load_dwordx4 v[20:23], v9, s[68:69]\n"
    "s_lshr_b32 s83, %9, 10\n"
    "s_mul_i32 s83, s83, 0x9e3779b1\n"
    "s_lshr_b32 s83, s83, 24\n"
    "s_mul_i32 s83, s83, 12\n"
    "s_lshr_b32 s83, s83, 4\n"
    "s_add_u32 s95, s95, 2133\n"
    "s_add_u32 s95, s95, s83\n"
    "s_mov_b64 s[40:41], %0\n"
    "s_mov_b32 s42, %3\n"
    "s_mov_b32 s43, 0x00020000\n"
    "s_mov_b32 s66, %6\n"
    "s_mov_b32 s67, 262144\n"
    "s_mov_b32 s88, %9\n"
    "s_mov_b32 s94, %10\n"
    "v_lshrrev_b32 v3, 4, v1\n"
    "v_bfe_u32 v0, v3, 2, 2\n"
    "v_lshlrev_b32 v0, 8, v0\n"
    "v_lshrrev_b32 v8, 4, v3\n"
    "v_lshl_add_u32 v0, v8, 6, v0\n"
    "v_and_b32 v9, 3, v3\n"
    "v_lshl_add_u32 v0, v9, 4, v0\n"
    "s_mov_b32 s70, 0x11111111\n"
    "s_mov_b32 s71, 0x11111111\n"
    "s_mov_b32 s72, 0x22222222\n"
    "s_mov_b32 s73, 0x22222222\n"
    "s_mov_b32 s74, 0x44444444\n"
    "s_mov_b32 s75, 0x44444444\n"
    "s_mov_b32 s76, 0x88888888\n"
    "s_mov_b32 s77, 0x88888888\n"
    "s_mov_b32 s78, 0x001f001f\n"
    "s_cmp_eq_u32 %3, 37632\n"
    "s_mov_b32 s79, 0x000f000f\n"
    "s_cselect_b32 s68, 16, 0\n"
    "s_or_b32 s79, s79, s68\n"
    "s_mov_b32 s80, 0xffff0000\n"
    "s_mov_b32 s81, -1\n"
    "s_mov_b32 s84, 0x3db504f3\n"
    "s_mov_b32 s85, 0x3fb8aa3b\n"
    "s_mov_b32 s86, 0xff800000\n"
    "s_mov_b32 s87, 0xffff0000\n"
    "v_mov_b32 v10, 0xffff\n"
    "v_mov_b32 v11, s87\n"
    "v_cndmask_b32 v12, v10, v11, s[76:77]\n"
    "v_cndmask_b32 v9, v9, 0, s[76:77]\n"
    "v_or_b32 v9, 12, v9\n"
    "v_mul_u32_u24 v34, 0x1111, v9\n"
    "s_mov_b32 vcc_lo, 0xf0f0f0f0\n"
    "s_mov_b32 vcc_hi, 0xf0f0f0f0\n"
    "v_cndmask_b32 v33, 0, v12, vcc\n"
    "v_xor_b32 v32, v33, v12\n"
    "s_mov_b32 vcc_lo, 0xaaaaaaaa\n"
    "s_mov_b32 vcc_hi, 0xaaaaaaaa\n"
    "v_cndmask_b32 v35, v10, v11, vcc\n"
    "v_and_b32 v9, 15, v3\n"
    "v_lshlrev_b32 v39, 5, v9\n"
    "v_and_b32 v9, 16, v3\n"
    "v_add3_u32 v39, v39, v9, %7\n"
    "v_mov_b32 v9, 0\n"
    "s_mov_b64 exec, 1\n"
    "ds_write_b32 v9, v9 offset:2080\n"
    "ds_write_b32 v9, v9 offset:4192\n"
    "ds_write_b32 v9, v9 offset:6304\n"
    "ds_write_b32 v9, v9 offset:8416\n"
    "s_mov_b64 exec, -1\n"
    "s_waitcnt lgkmcnt(0)\n"
    "s_barrier\n"
    "s_add_u32 s92, s92, 1\n"
    "s_addc_u32 s93, s93, 0\n"
    "s_branch Lqad_%=\n"
    "Lend_%=:\n"
    "s_waitcnt vmcnt(0) lgkmcnt(0)\n"
    :: "s"(kb), "s"(vb), "s"(a.ws), "s"(nbytes), "s"(ring), "s"(qoff), "s"(rec), "s"(mw), "s"(w), "s"(st), "s"(WS_F + head * 128)
    : ALL_REGS);
  }
}

extern "C" void kernel_launch(void* const* d_in, const int* in_sizes, int n_in, void* d_out, int out_size, void* d_ws, size_t ws_size, hipStream_t stream) {
  Args a;
  a.x = (const u16*)d_in[0]; a.k = (const u16*)d_in[1]; a.v = (const u16*)d_in[2]; a.wq = (const u16*)d_in[3]; a.wo = (const u16*)d_in[4];
  a.wg = (const u16*)d_in[6]; a.wu = (const u16*)d_in[7]; a.wd = (const u16*)d_in[8]; a.out = (u16*)d_out; a.ws = (unsigned char*)d_ws;
  fused_decode<<<256, 256, 0, stream>>>(a);
}
```
